```python
import torch

def _softmax(x, axis=-1):
    return torch.softmax(x, dim=axis)
def _gelu(x):
    return torch.nn.functional.gelu(x, approximate="tanh")
def _silu(x):
    return torch.nn.functional.silu(x)
def _sigmoid(x):
    return torch.sigmoid(x)
def _layernorm(x, axis=-1, eps=1e-5):
    m = x.mean(dim=axis, keepdim=True)
    v = x.var(dim=axis, keepdim=True, unbiased=False)
    return (x - m) / torch.sqrt(v + eps)
def _scatter_add(tgt, idx, src, axis=0):
    return tgt.clone().index_add_(axis, idx, src)
def _segment_sum(src, seg, n):
    out = torch.zeros((n,) + src.shape[1:], dtype=src.dtype)
    return out.index_add_(0, seg, src)
def _pad(x, pw):
    flat = tuple(v for pair in reversed(pw) for v in pair)
    return torch.nn.functional.pad(x, flat)

kernel_name = 'gated_mix__ssm_conv_s4__dense_activation_squared_relu__prompt'

def setup_inputs():

    _w = (lambda a: (lambda t: t.to(torch.bfloat16) if t.is_floating_point() else t)(torch.as_tensor(a)))
    def _A_setup():
        B, L, D, N = 1, 2048, 512, 128
        A_re = -(lambda _x: torch.exp(torch.as_tensor(_x)))((lambda *s: torch.randn(*s, dtype=torch.bfloat16) * 6.4)(D, N))
        A_im = torch.pi * (lambda *s: torch.randn(*s, dtype=torch.bfloat16) * 0.1)(D, N)
        B_re = (lambda *s: torch.randn(*s, dtype=torch.bfloat16) * 0.2)(D, N)
        B_im = (lambda *s: torch.randn(*s, dtype=torch.bfloat16) * 0.2)(D, N)
        C_re = (lambda *s: torch.randn(*s, dtype=torch.bfloat16) * 0.2)(D, N)
        C_im = (lambda *s: torch.randn(*s, dtype=torch.bfloat16) * 0.2)(D, N)
        log_dt = (lambda *s: torch.randn(*s, dtype=torch.bfloat16) * 0.1)(D) * 0.5 - 4.0
        Dskip = (lambda *s: torch.randn(*s, dtype=torch.bfloat16) * 0.1)(D)
        return {
            "u": _w((lambda *s: torch.randn(*s, dtype=torch.bfloat16) * 0.05)(B, L, D)),
            "A_re": _w(A_re),
            "A_im": _w(A_im),
            "B_re": _w(B_re),
            "B_im": _w(B_im),
            "C_re": _w(C_re),
            "C_im": _w(C_im),
            "log_dt": _w(log_dt),
            "Dskip": _w(Dskip),
        }
    _A_in = _A_setup()

    def _B_setup():
        B, S, D, F = 1, 2048, 512, 1792
        return {
            "x": _w((lambda *s: torch.randn(*s, dtype=torch.bfloat16) * 0.05)(B, S, D)),
            "w1": _w((lambda *s: torch.randn(*s, dtype=torch.bfloat16) * 0.05)(D, F)),
            "b1": _w((lambda *s: torch.randn(*s, dtype=torch.bfloat16) * 0.05)(F)),
            "w2": _w((lambda *s: torch.randn(*s, dtype=torch.bfloat16) * 0.05)(F, D)),
            "b2": _w((lambda *s: torch.randn(*s, dtype=torch.bfloat16) * 0.05)(D)),
        }
    _B_in = _B_setup()

    return {
        "A_u": _A_in["u"],
        "A_A_re": _A_in["A_re"],
        "A_A_im": _A_in["A_im"],
        "A_B_re": _A_in["B_re"],
        "A_B_im": _A_in["B_im"],
        "A_C_re": _A_in["C_re"],
        "A_C_im": _A_in["C_im"],
        "A_log_dt": _A_in["log_dt"],
        "A_Dskip": _A_in["Dskip"],
        "B_x": _B_in["x"],
        "B_w1": _B_in["w1"],
        "B_b1": _B_in["b1"],
        "B_w2": _B_in["w2"],
        "B_b2": _B_in["b2"],
        "gate_w": _w((lambda *s: torch.randn(*s, dtype=torch.bfloat16) * 0.05)(512) * 16.0),
    }

def reference(A_u, A_A_re, A_A_im, A_B_re, A_B_im, A_C_re, A_C_im, A_log_dt, A_Dskip, B_x, B_w1, B_b1, B_w2, B_b2, gate_w):
    def _A_ref(u, A_re, A_im, B_re, B_im, C_re, C_im, log_dt, Dskip):
        Bsz, L, D = u.shape
        dt = torch.exp(log_dt)[:, None]
        A = A_re + 1j * A_im
        Bc = B_re + 1j * B_im
        Cc = C_re + 1j * C_im
        logdA = dt * A
        dB = dt * Bc
        l = (lambda x, dt: x.to(dt))(torch.arange(L), A_re.dtype)
        P = torch.exp(logdA[:, :, None] * l[None, None, :])
        K = torch.einsum("dn,dnl,dn->dl", Cc, P, dB).real
        L2 = 2 * L
        Kp = _pad(K, ((0, 0), (0, L)))
        up = (lambda x, *d: x.permute(*d))(u, 0, 2, 1)
        up = _pad(up, ((0, 0), (0, 0), (0, L)))
        Kf = (lambda x, axes=(-2,-1): torch.fft.rfft2(x.float(), dim=axes))(Kp[:, None, :], axes=(-2, -1))
        Uf = (lambda x, axes=(-2,-1): torch.fft.rfft2(x.float(), dim=axes))(up[:, :, None, :], axes=(-2, -1))
        Yf = Uf * Kf[None, :, :, :]
        y = (lambda x, s, axes=(-2,-1): torch.fft.irfft2(x, s=s, dim=axes).to(torch.bfloat16))(Yf, (1, L2), axes=(-2, -1))[:, :, 0, :L]
        y = (lambda x, *d: x.permute(*d))(y, 0, 2, 1) + u * Dskip[None, None, :]
        return y

    def _B_ref(x, w1, b1, w2, b2):
        h = torch.einsum("bsd,df->bsf", x, w1) + b1
        zero = torch.zeros_like(h)
        r = torch.where(h > zero, h, zero)
        h = r * r
        out = torch.einsum("bsf,fd->bsd", h, w2) + b2
        return out

    x = A_u
    g = _sigmoid((x * gate_w).sum(-1, keepdims=True))
    a = _A_ref(x, A_A_re, A_A_im, A_B_re, A_B_im, A_C_re, A_C_im, A_log_dt, A_Dskip)
    b = _B_ref(x, B_w1, B_b1, B_w2, B_b2)
    return g * a + (1 - g) * b
```

```cpp
#include <hip/hip_runtime.h>
#include <hip/hip_bf16.h>
#include <stdint.h>

constexpr int L = 2048, D = 512, N = 128, F = 1792;
typedef unsigned short u16;
__device__ __forceinline__ float bf2f(u16 x) { return __uint_as_float(((unsigned)x) << 16); }
__device__ __forceinline__ u16 f2bf(float f) { __hip_bfloat16 b = __float2bfloat16(f); return *reinterpret_cast<u16*>(&b); }
__device__ __forceinline__ float rbf(float f) { return bf2f(f2bf(f)); }

__global__ void kgen(const u16* Are, const u16* Aim, const u16* Bre, const u16* Bim, const u16* Cre, const u16* Cim,
                     const u16* logdt, float* K) {
    int d = blockIdx.x;
    float dt = rbf(expf(bf2f(logdt[d])));
    for (int l = threadIdx.x; l < L; l += blockDim.x) {
        float lf = rbf((float)l);
        float acc = 0.f;
        for (int n = 0; n < N; ++n) {
            int i = d * N + n;
            float zr = dt * bf2f(Are[i]), zi = dt * bf2f(Aim[i]);
            float br = dt * bf2f(Bre[i]), bi = dt * bf2f(Bim[i]);
            float cr = bf2f(Cre[i]), ci = bf2f(Cim[i]);
            float wr = cr * br - ci * bi, wi = cr * bi + ci * br;
            float e = expf(zr * lf), s, c;
            sincosf(zi * lf, &s, &c);
            acc += e * (wr * c - wi * s);
        }
        K[d * L + l] = acc;
    }
}
__global__ void conv(const u16* u, const float* K, float* y) {
    int d = blockIdx.x;
    __shared__ float su[L], sk[L];
    for (int t = threadIdx.x; t < L; t += blockDim.x) { su[t] = bf2f(u[t * D + d]); sk[t] = K[d * L + t]; }
    __syncthreads();
    for (int t = threadIdx.x; t < L; t += blockDim.x) {
        float acc = 0.f;
        for (int j = 0; j <= t; ++j) acc += sk[j] * su[t - j];
        y[t * D + d] = acc;
    }
}
__global__ void gemm1(const u16* x, const u16* w1, const u16* b1, u16* h) {
    int t = blockIdx.x;
    __shared__ float sx[D];
    for (int i = threadIdx.x; i < D; i += blockDim.x) sx[i] = bf2f(x[t * D + i]);
    __syncthreads();
    for (int f = threadIdx.x; f < F; f += blockDim.x) {
        float acc = 0.f;
        for (int k = 0; k < D; ++k) acc += sx[k] * bf2f(w1[k * F + f]);
        float v = rbf(rbf(acc) + bf2f(b1[f]));
        v = v > 0.f ? v : 0.f;
        h[t * F + f] = f2bf(v * v);
    }
}
__global__ void gemm2(const u16* x, const u16* h, const u16* w2, const u16* b2, const float* y, const u16* Dskip,
                      const u16* gw, u16* out) {
    int t = blockIdx.x;
    __shared__ float sh[F];
    __shared__ float sg;
    for (int i = threadIdx.x; i < F; i += blockDim.x) sh[i] = bf2f(h[t * F + i]);
    if (threadIdx.x < 64) {
        float s = 0.f;
        for (int i = threadIdx.x; i < D; i += 64) s += rbf(bf2f(x[t * D + i]) * bf2f(gw[i]));
        for (int m = 32; m >= 1; m >>= 1) s += __shfl_xor(s, m);
        if (threadIdx.x == 0) { float sv = rbf(s); sg = rbf(1.f / (1.f + expf(-sv))); }
    }
    __syncthreads();
    float g = sg;
    for (int d = threadIdx.x; d < D; d += blockDim.x) {
        float acc = 0.f;
        for (int k = 0; k < F; ++k) acc += sh[k] * bf2f(w2[k * D + d]);
        float b = rbf(rbf(acc) + bf2f(b2[d]));
        float a = rbf(rbf(y[t * D + d]) + rbf(bf2f(x[t * D + d]) * bf2f(Dskip[d])));
        out[t * D + d] = f2bf(rbf(g * a) + rbf(rbf(1.f - g) * b));
    }
}

extern "C" void kernel_launch(void* const* d_in, const int* in_sizes, int n_in,
                              void* d_out, int out_size, void* d_ws, size_t ws_size,
                              hipStream_t stream) {
    const u16* u = (const u16*)d_in[0];
    float* K = (float*)d_ws;
    float* y = K + D * L;
    u16* h = (u16*)(y + L * D);
    kgen<<<D, 256, 0, stream>>>((const u16*)d_in[1], (const u16*)d_in[2], (const u16*)d_in[3], (const u16*)d_in[4],
                                (const u16*)d_in[5], (const u16*)d_in[6], (const u16*)d_in[7], K);
    conv<<<D, 256, 0, stream>>>(u, K, y);
    gemm1<<<L, 256, 0, stream>>>(u, (const u16*)d_in[10], (const u16*)d_in[11], h);
    gemm2<<<L, 256, 0, stream>>>(u, h, (const u16*)d_in[12], (const u16*)d_in[13], y, (const u16*)d_in[8],
                                 (const u16*)d_in[14], (u16*)d_out);
}
```

```cpp
#include <hip/hip_runtime.h>
#include <hip/hip_bf16.h>
#include <stdint.h>

constexpr int L = 2048, D = 512, N = 128, F = 1792;
typedef unsigned short u16;
__device__ __forceinline__ float bf2f(u16 x) { return __uint_as_float(((unsigned)x) << 16); }
__device__ __forceinline__ u16 f2bf(float f) { __hip_bfloat16 b = __float2bfloat16(f); return *reinterpret_cast<u16*>(&b); }
__device__ __forceinline__ float rbf(float f) { return bf2f(f2bf(f)); }

__global__ void kgen(const u16* Are, const u16* Aim, const u16* Bre, const u16* Bim, const u16* Cre, const u16* Cim,
                     const u16* logdt, float* K) {
    int d = blockIdx.x;
    float dt = rbf(expf(bf2f(logdt[d])));
    for (int l = threadIdx.x; l < L; l += blockDim.x) {
        float lf = rbf((float)l);
        float acc = 0.f;
        for (int n = 0; n < N; ++n) {
            int i = d * N + n;
            float zr = dt * bf2f(Are[i]), zi = dt * bf2f(Aim[i]);
            float br = dt * bf2f(Bre[i]), bi = dt * bf2f(Bim[i]);
            float cr = bf2f(Cre[i]), ci = bf2f(Cim[i]);
            float wr = cr * br - ci * bi, wi = cr * bi + ci * br;
            float e = expf(zr * lf), s, c;
            sincosf(zi * lf, &s, &c);
            acc += e * (wr * c - wi * s);
        }
        K[d * L + l] = acc;
    }
}
__global__ void conv(const u16* u, const float* K, float* y) {
    int d = blockIdx.x;
    __shared__ float su[L], sk[L];
    for (int t = threadIdx.x; t < L; t += blockDim.x) { su[t] = bf2f(u[t * D + d]); sk[t] = K[d * L + t]; }
    __syncthreads();
    for (int t = threadIdx.x; t < L; t += blockDim.x) {
        float acc = 0.f;
        for (int j = 0; j <= t; ++j) acc += sk[j] * su[t - j];
        y[t * D + d] = acc;
    }
}
__global__ void gemm1(const u16* x, const u16* w1, const u16* b1, u16* h) {
    int t = blockIdx.x;
    __shared__ float sx[D];
    for (int i = threadIdx.x; i < D; i += blockDim.x) sx[i] = bf2f(x[t * D + i]);
    __syncthreads();
    for (int f = threadIdx.x; f < F; f += blockDim.x) {
        float acc = 0.f;
        for (int k = 0; k < D; ++k) acc += sx[k] * bf2f(w1[k * F + f]);
        float v = rbf(rbf(acc) + bf2f(b1[f]));
        v = v > 0.f ? v : 0.f;
        h[t * F + f] = f2bf(v * v);
    }
}
__global__ void gemm2(const u16* x, const u16* h, const u16* w2, const u16* b2, const float* y, const u16* Dskip,
                      const u16* gw, u16* out) {
    int t = blockIdx.x;
    __shared__ float sh[F];
    __shared__ float sg;
    for (int i = threadIdx.x; i < F; i += blockDim.x) sh[i] = bf2f(h[t * F + i]);
    if (threadIdx.x < 64) {
        float s = 0.f;
        for (int i = threadIdx.x; i < D; i += 64) s += rbf(bf2f(x[t * D + i]) * bf2f(gw[i]));
        for (int m = 32; m >= 1; m >>= 1) s += __shfl_xor(s, m);
        if (threadIdx.x == 0) { float sv = rbf(s); sg = rbf(1.f / (1.f + expf(-sv))); }
    }
    __syncthreads();
    float g = sg;
    for (int d = threadIdx.x; d < D; d += blockDim.x) {
        float acc = 0.f;
        for (int k = 0; k < F; ++k) acc += sh[k] * bf2f(w2[k * D + d]);
        float b = rbf(rbf(acc) + bf2f(b2[d]));
        float a = rbf(rbf(y[t * D + d]) + rbf(bf2f(x[t * D + d]) * bf2f(Dskip[d])));
        out[t * D + d] = f2bf(rbf(g * a) + rbf(rbf(1.f - g) * b));
    }
}


__global__ void __launch_bounds__(64) longcode(unsigned* sink) {
    unsigned v = threadIdx.x;
#pragma unroll 1
    for (int it = 0; it < 56; ++it)
        asm volatile(".p2align 3\n .rept 128\n v_add_u32_e64 %0, %0, %0\n .endr\n" : "+v"(v));
    if (v == 0x12345678u) sink[0] = v;
}
extern "C" void kernel_launch(void* const* d_in, const int* in_sizes, int n_in,
                              void* d_out, int out_size, void* d_ws, size_t ws_size,
                              hipStream_t stream) {
    const u16* u = (const u16*)d_in[0];
    float* K = (float*)d_ws;
    float* y = K + D * L;
    u16* h = (u16*)(y + L * D);
    longcode<<<256, 64, 0, stream>>>((unsigned*)d_ws + (60 << 20));
    kgen<<<D, 256, 0, stream>>>((const u16*)d_in[1], (const u16*)d_in[2], (const u16*)d_in[3], (const u16*)d_in[4],
                                (const u16*)d_in[5], (const u16*)d_in[6], (const u16*)d_in[7], K);
    conv<<<D, 256, 0, stream>>>(u, K, y);
    gemm1<<<L, 256, 0, stream>>>(u, (const u16*)d_in[10], (const u16*)d_in[11], h);
    gemm2<<<L, 256, 0, stream>>>(u, h, (const u16*)d_in[12], (const u16*)d_in[13], y, (const u16*)d_in[8],
                                 (const u16*)d_in[14], (u16*)d_out);
}
```

```cpp
#include <hip/hip_runtime.h>
#include <stdint.h>

struct Srd { const void* p; unsigned n; unsigned f; };
struct Prm {
    Srd x, w1, w2, ws;
    const void* b1; void* out;
    const void* ar; const void* ai; const void* br; const void* bi; const void* cr; const void* ci;
    const void* ldt; const void* dsk; const void* b2; const void* gw;
};
constexpr unsigned WS_USED = 0x1100000;

#define MK_ASM \
    "s_getpc_b64 s[40:41]\n" \
    "Lpc:\n" \
    "v_readfirstlane_b32 s36, %2\n" \
    "s_load_dwordx16 s[12:27], %0, 0\n" \
    "v_and_b32 v1, 0x1e0, %2\n" \
    "s_and_b32 s37, %1, -8\n" \
    "s_lshl_b32 s37, s37, 8\n" \
    "s_mov_b32 m0, 163456\n" \
    "v_lshl_add_u32 v1, v1, 2, s37\n" \
    "v_min_u32 v1, Lcend-Lpc, v1\n" \
    "s_lshr_b32 s36, s36, 6\n" \
    "global_load_lds_dword v1, s[40:41]\n" \
    "v_and_b32 v255, 63, %2\n" \
    "s_mov_b32 s10, %1\n" \
    "s_cmp_ge_u32 s36, 4\n" \
    "s_cbranch_scc0 Lg\n" \
    "s_load_dwordx8 s[44:51], %0, 80\n" \
    "s_load_dwordx4 s[52:55], %0, 112\n" \
    "s_load_dwordx8 s[56:63], %0, 128\n" \
    "s_load_dwordx2 s[64:65], %0, 72\n" \
    "v_lshlrev_b32 v1, 2, v255\n" \
    "v_and_b32 v6, 0x7c, v1\n" \
    "s_bfe_u32 s4, s36, 0x10001\n" \
    "s_mul_i32 s4, s4, 16128\n" \
    "s_add_u32 s28, s4, 131072\n" \
    "s_and_b32 s30, s36, 1\n" \
    "s_and_b32 s31, s36, 3\n" \
    "s_mul_i32 s6, s30, 4352\n" \
    "s_add_u32 s29, s6, s28\n" \
    "s_lshl2_add_u32 s7, %1, s31\n" \
    "s_lshl_b32 s7, s7, 7\n" \
    "v_add_u32 v6, s7, v6\n" \
    "s_mov_b32 m0, s29\n" \
    "s_lshl_b32 s8, %1, 2\n" \
    "v_add_u32 v7, 163328, v1\n" \
    "v_mov_b32 v2, 0\n" \
    "v_mov_b32 v3, 0\n" \
    "v_mov_b32 v4, 0\n" \
    "v_mov_b32 v5, 0\n" \
    "s_lshl_b32 s5, s30, 11\n" \
    "ds_write_b32 v7, v2\n" \
    "s_add_u32 s4, s28, s5\n" \
    "s_add_u32 s4, s4, 8688\n" \
    "v_lshl_add_u32 v8, v255, 4, s4\n" \
    "ds_write_b128 v8, v[2:5]\n" \
    "ds_write_b128 v8, v[2:5] offset:1024\n" \
    "s_waitcnt lgkmcnt(0)\n" \
    "global_load_lds_dword v6, s[44:45]\n" \
    "s_add_u32 m0, m0, 256\n" \
    "s_load_dword s38, s[56:57], s8\n" \
    "global_load_lds_dword v6, s[46:47]\n" \
    "s_add_u32 m0, m0, 256\n" \
    "s_load_dword s39, s[58:59], s8\n" \
    "global_load_lds_dword v6, s[48:49]\n" \
    "s_add_u32 m0, m0, 256\n" \
    "v_and_b32 v7, 4, v1\n" \
    "global_load_lds_dword v6, s[50:51]\n" \
    "s_add_u32 m0, m0, 256\n" \
    "v_add_u32 v7, 7831552, v7\n" \
    "global_load_lds_dword v6, s[52:53]\n" \
    "s_add_u32 m0, m0, 256\n" \
    "s_nop 0\n" \
    "global_load_lds_dword v6, s[54:55]\n" \
    "s_add_u32 m0, m0, 256\n" \
    "s_cmp_eq_u32 s31, 0\n" \
    "s_cbranch_scc0 Lnoep\n" \
    "global_load_lds_dword v7, s[24:25] sc1\n" \
    "Lnoep:\n" \
    "s_barrier\n" \
    "s_branch Lssm\n" \
    "Lg:\n" \
    "s_cmp_ge_u32 %1, 224\n" \
    "s_cbranch_scc0 Lgm\n" \
    "s_load_dwordx2 s[38:39], %0, 152\n" \
    "s_and_b32 s4, %1, 6\n" \
    "s_lshr_b32 s5, %1, 3\n" \
    "s_lshl1_add_u32 s4, s4, s5\n" \
    "s_sub_u32 s75, s4, 28\n" \
    "s_lshl_b32 s4, s75, 17\n" \
    "s_and_b32 s5, %1, 1\n" \
    "s_lshl2_add_u32 s76, s5, s36\n" \
    "s_lshl_b32 s5, s76, 7\n" \
    "s_add_u32 s4, s4, s5\n" \
    "v_lshrrev_b32 v1, 3, v255\n" \
    "v_and_b32 v2, 7, v255\n" \
    "v_lshrrev_b32 v3, 2, v255\n" \
    "v_and_b32 v3, 4, v3\n" \
    "v_xor_b32 v2, v2, v3\n" \
    "v_lshlrev_b32 v1, 10, v1\n" \
    "v_lshl_add_u32 v1, v2, 4, v1\n" \
    "s_lshl_b32 s72, s36, 14\n" \
    "s_add_u32 s72, s72, 65536\n" \
    "s_mov_b32 m0, s72\n" \
    "s_waitcnt lgkmcnt(0)\n" \
    "buffer_load_dwordx4 v1, s[12:15], s4 offen lds\n" \
    "s_add_u32 m0, m0, 1024\n" \
    "s_add_u32 s4, s4, 8192\n" \
    "buffer_load_dwordx4 v1, s[12:15], s4 offen lds\n" \
    "s_add_u32 m0, m0, 1024\n" \
    "s_add_u32 s4, s4, 8192\n" \
    "buffer_load_dwordx4 v1, s[12:15], s4 offen lds\n" \
    "s_add_u32 m0, m0, 1024\n" \
    "s_add_u32 s4, s4, 8192\n" \
    "buffer_load_dwordx4 v1, s[12:15], s4 offen lds\n" \
    "s_add_u32 m0, m0, 1024\n" \
    "s_add_u32 s4, s4, 8192\n" \
    "s_branch Lhx\n" \
    "Lgm:\n" \
    "s_load_dwordx2 s[38:39], %0, 64\n" \
    "s_lshr_b32 s4, %1, 3\n" \
    "s_mul_i32 s5, s4, 37\n" \
    "s_lshr_b32 s5, s5, 8\n" \
    "s_mul_i32 s6, s5, 7\n" \
    "s_sub_u32 s6, s4, s6\n" \
    "s_bfe_u32 s7, %1, 0x20001\n" \
    "s_lshl2_add_u32 s75, s7, s5\n" \
    "s_and_b32 s7, %1, 1\n" \
    "s_mul_i32 s7, s7, 7\n" \
    "s_add_u32 s76, s7, s6\n" \
    "v_lshrrev_b32 v1, 3, v255\n" \
    "v_and_b32 v2, 7, v255\n" \
    "v_lshrrev_b32 v3, 4, v255\n" \
    "v_lshlrev_b32 v4, 2, v3\n" \
    "v_and_b32 v4, 4, v4\n" \
    "v_xor_b32 v4, v4, v2\n" \
    "v_mul_u32_u24 v5, 0xe00, v1\n" \
    "s_lshl_b32 s9, s76, 8\n" \
    "v_lshl_add_u32 v4, v4, 4, s9\n" \
    "s_mul_i32 s9, s36, 28672\n" \
    "v_add3_u32 v226, v4, v5, s9\n" \
    "v_add_u32 v254, 114688, v226\n" \
    "s_mul_i32 s9, s5, 0\n" \
    "s_mul_i32 s8, s6, 0\n" \
    "s_add_u32 s9, s9, s8\n" \
    "s_bfe_u32 s8, %1, 0x10001\n" \
    "s_mul_i32 s8, s8, 64\n" \
    "s_add_u32 s9, s9, s8\n" \
    "s_and_b32 s30, s9, 112\n" \
    "s_lshl_b32 s33, s36, 10\n" \
    "v_xor_b32 v3, v3, v2\n" \
    "v_lshlrev_b32 v5, 10, v1\n" \
    "v_lshl_add_u32 v224, v3, 4, v5\n" \
    "v_xor_b32 v3, 64, v224\n" \
    "v_add_u32 v225, 8192, v3\n" \
    "v_add_u32 v124, 16384, v224\n" \
    "v_add_u32 v125, 16384, v225\n" \
    "s_lshl2_add_u32 s4, s75, s36\n" \
    "s_lshl_b32 s31, s4, 15\n" \
    "s_lshl_b32 s72, s36, 14\n" \
    "s_add_u32 s72, s72, 65536\n" \
    "s_setprio 2\n" \
    "s_waitcnt lgkmcnt(0)\n" \
    "s_mov_b32 m0, s33\n" \
    "s_mul_i32 s50, s30, 14336\n" \
    "s_lshl3_add_u32 s42, s30, s31\n" \
    "buffer_load_dwordx4 v226, s[16:19], s50 offen lds\n" \
    "s_mov_b32 m0, s72\n" \
    "s_nop 0\n" \
    "buffer_load_dwordx4 v224, s[12:15], s42 offen lds\n" \
    "s_add_u32 m0, s33, 4096\n" \
    "s_nop 0\n" \
    "buffer_load_dwordx4 v254, s[16:19], s50 offen lds\n" \
    "s_add_u32 m0, s72, 1024\n" \
    "s_nop 0\n" \
    "buffer_load_dwordx4 v225, s[12:15], s42 offen lds\n" \
    "s_add_u32 m0, s72, 2048\n" \
    "s_nop 0\n" \
    "buffer_load_dwordx4 v124, s[12:15], s42 offen lds\n" \
    "s_add_u32 m0, s72, 3072\n" \
    "s_nop 0\n" \
    "buffer_load_dwordx4 v125, s[12:15], s42 offen lds\n" \
    "v_and_b32 v1, 31, v255\n" \
    "s_lshl_b32 s9, s76, 8\n" \
    "v_lshl_add_u32 v6, v1, 1, s9\n" \
    "s_barrier\n" \
    "s_lshr_b32 s9, s10, 3\n" \
    "s_add_u32 s34, s9, s10\n" \
    "s_and_b32 s34, s34, 3\n" \
    "s_lshl2_add_u32 s9, s9, s36\n" \
    "s_lshl_b32 s9, s9, 14\n" \
    "s_add_u32 s9, s9, 384\n" \
    "v_lshl_add_u32 v117, v255, 10, s9\n" \
    "s_mov_b32 s73, 0\n" \
    "v_bfrev_b32 v116, 1\n" \
    "v_cmp_gt_u32 vcc, 16, v255\n" \
    "s_nop 1\n" \
    "v_cndmask_b32 v117, v116, v117, vcc\n" \
    "v_lshl_add_u32 v1, v1, 7, s72\n" \
    "v_bfe_u32 v2, v255, 1, 3\n" \
    "v_lshrrev_b32 v3, 5, v255\n" \
    "v_xor_b32 v2, v2, v3\n" \
    "v_lshl_add_u32 v228, v2, 4, v1\n" \
    "v_xor_b32 v229, 32, v228\n" \
    "v_xor_b32 v230, 64, v228\n" \
    "v_xor_b32 v231, 96, v228\n" \
    "v_bfe_u32 v1, v255, 2, 2\n" \
    "v_lshl_add_u32 v2, v3, 3, v1\n" \
    "v_lshlrev_b32 v2, 7, v2\n" \
    "v_bfe_u32 v3, v255, 4, 1\n" \
    "v_bfe_u32 v4, v255, 1, 1\n" \
    "v_lshl_add_u32 v3, v3, 1, v4\n" \
    "v_and_b32 v1, 2, v1\n" \
    "v_lshlrev_b32 v1, 1, v1\n" \
    "v_xor_b32 v3, v3, v1\n" \
    "v_lshl_add_u32 v2, v3, 4, v2\n" \
    "v_and_b32 v1, 1, v255\n" \
    "v_lshl_add_u32 v232, v1, 3, v2\n" \
    "v_xor_b32 v233, 64, v232\n" \
    "v_and_b32 v1, 3, v255\n" \
    "v_lshlrev_b32 v1, 2, v1\n" \
    "v_add_u32 v238, 163328, v1\n" \
    "s_lshl_b32 s8, s36, 2\n" \
    "s_add_u32 s8, s8, 163328\n" \
    "v_mov_b32 v241, s8\n" \
    "v_lshlrev_b32 v1, 2, v255\n" \
    "v_add_u32 v1, 163456, v1\n" \
    "v_cmp_eq_u32 vcc, 0, v255\n" \
    "s_mov_b32 s70, 0x20000\n" \
    "s_lshl_b32 s71, s36, 12\n" \
    "v_cndmask_b32 v239, v1, v241, vcc\n" \
    "v_mov_b32 v97, 0\n" \
    "v_mov_b32 v98, 0\n" \
    "v_mov_b32 v99, 0\n" \
    "v_mov_b32 v101, 0\n" \
    "v_mov_b32 v102, 0\n" \
    "v_mov_b32 v103, 0\n" \
    "v_mov_b32 v105, 0\n" \
    "v_mov_b32 v106, 0\n" \
    "v_mov_b32 v107, 0\n" \
    "v_mov_b32 v109, 0\n" \
    "v_mov_b32 v110, 0\n" \
    "v_mov_b32 v111, 0\n" \
    "v_mov_b32 v113, 0\n" \
    "v_mov_b32 v114, 0\n" \
    "v_mov_b32 v115, 0\n" \
    "v_mov_b32 v121, 0x3f80\n" \
    "v_cmp_gt_u32 vcc, 32, v255\n" \
    "s_nop 1\n" \
    "v_cndmask_b32 v112, 0, v121, vcc\n" \
    "global_load_ushort v96, v6, s[38:39]\n" \
    "global_load_ushort v100, v6, s[38:39] offset:64\n" \
    "global_load_ushort v104, v6, s[38:39] offset:128\n" \
    "global_load_ushort v108, v6, s[38:39] offset:192\n" \
    "s_lshr_b32 s6, s10, 3\n" \
    "s_mul_i32 s7, s6, 37\n" \
    "s_lshr_b32 s7, s7, 8\n" \
    "s_mul_i32 s8, s7, 7\n" \
    "s_sub_u32 s8, s6, s8\n" \
    "s_cmp_ge_u32 s8, 3\n" \
    "s_addc_u32 s5, s8, 0\n" \
    "s_cmp_eq_u32 s7, 4\n" \
    "s_cselect_b32 s5, 3, s5\n" \
    "s_cselect_b32 s7, s8, s7\n" \
    "s_cselect_b32 s8, 3, s8\n" \
    "s_and_b32 s4, s10, 7\n" \
    "s_lshl2_add_u32 s4, s4, s7\n" \
    "s_add_u32 m0, s33, 8192\n" \
    "s_add_u32 s30, s30, 16\n" \
    "s_and_b32 s30, s30, 0x70\n" \
    "s_mul_i32 s51, s30, 14336\n" \
    "s_lshl3_add_u32 s43, s30, s31\n" \
    "buffer_load_dwordx4 v226, s[16:19], s51 offen lds\n" \
    "s_mov_b32 s6, 0\n" \
    "s_mul_i32 s7, s36, 7\n" \
    "s_sub_u32 s9, s6, 7\n" \
    "s_cselect_b32 s8, s6, s9\n" \
    "s_lshl_b32 s63, s8, 11\n" \
    "s_add_u32 s8, s8, s7\n" \
    "s_and_b32 s9, s8, 1\n" \
    "s_mul_i32 s9, s9, 0x8000\n" \
    "s_lshl_b32 s8, s8, 16\n" \
    "s_sub_u32 s56, s8, s9\n" \
    "s_add_u32 s6, s6, 1\n" \
    "s_add_u32 m0, s33, 12288\n" \
    "s_nop 0\n" \
    "buffer_load_dwordx4 v254, s[16:19], s51 offen lds\n" \
    "s_sub_u32 s9, s6, 7\n" \
    "s_cselect_b32 s8, s6, s9\n" \
    "s_lshl_b32 s64, s8, 11\n" \
    "s_add_u32 s8, s8, s7\n" \
    "s_and_b32 s9, s8, 1\n" \
    "s_mul_i32 s9, s9, 0x8000\n" \
    "s_lshl_b32 s8, s8, 16\n" \
    "s_sub_u32 s57, s8, s9\n" \
    "s_add_u32 s6, s6, 1\n" \
    "s_sub_u32 s9, s6, 7\n" \
    "s_cselect_b32 s8, s6, s9\n" \
    "s_lshl_b32 s65, s8, 11\n" \
    "s_add_u32 m0, s72, 4096\n" \
    "s_nop 0\n" \
    "buffer_load_dwordx4 v224, s[12:15], s43 offen lds\n" \
    "s_add_u32 s8, s8, s7\n" \
    "s_and_b32 s9, s8, 1\n" \
    "s_mul_i32 s9, s9, 0x8000\n" \
    "s_lshl_b32 s8, s8, 16\n" \
    "s_sub_u32 s58, s8, s9\n" \
    "s_add_u32 s6, s6, 1\n" \
    "s_sub_u32 s9, s6, 7\n" \
    "s_cselect_b32 s8, s6, s9\n" \
    "s_lshl_b32 s66, s8, 11\n" \
    "s_add_u32 s8, s8, s7\n" \
    "s_add_u32 m0, s33, 16384\n" \
    "s_add_u32 s30, s30, 16\n" \
    "s_and_b32 s30, s30, 0x70\n" \
    "s_mul_i32 s52, s30, 14336\n" \
    "s_lshl3_add_u32 s44, s30, s31\n" \
    "buffer_load_dwordx4 v226, s[16:19], s52 offen lds\n" \
    "s_and_b32 s9, s8, 1\n" \
    "s_mul_i32 s9, s9, 0x8000\n" \
    "s_lshl_b32 s8, s8, 16\n" \
    "s_sub_u32 s59, s8, s9\n" \
    "s_add_u32 s6, s6, 1\n" \
    "s_sub_u32 s9, s6, 7\n" \
    "s_cselect_b32 s8, s6, s9\n" \
    "s_lshl_b32 s67, s8, 11\n" \
    "s_add_u32 s8, s8, s7\n" \
    "s_and_b32 s9, s8, 1\n" \
    "s_mul_i32 s9, s9, 0x8000\n" \
    "s_add_u32 m0, s72, 5120\n" \
    "s_nop 0\n" \
    "buffer_load_dwordx4 v225, s[12:15], s43 offen lds\n" \
    "s_lshl_b32 s8, s8, 16\n" \
    "s_sub_u32 s60, s8, s9\n" \
    "s_add_u32 s6, s6, 1\n" \
    "s_sub_u32 s9, s6, 7\n" \
    "s_cselect_b32 s8, s6, s9\n" \
    "s_lshl_b32 s68, s8, 11\n" \
    "s_add_u32 s8, s8, s7\n" \
    "s_and_b32 s9, s8, 1\n" \
    "s_mul_i32 s9, s9, 0x8000\n" \
    "s_lshl_b32 s8, s8, 16\n" \
    "s_sub_u32 s61, s8, s9\n" \
    "s_add_u32 m0, s33, 20480\n" \
    "s_nop 0\n" \
    "buffer_load_dwordx4 v254, s[16:19], s52 offen lds\n" \
    "s_add_u32 s6, s6, 1\n" \
    "s_sub_u32 s9, s6, 7\n" \
    "s_cselect_b32 s8, s6, s9\n" \
    "s_lshl_b32 s69, s8, 11\n" \
    "s_add_u32 s8, s8, s7\n" \
    "s_and_b32 s9, s8, 1\n" \
    "s_mul_i32 s9, s9, 0x8000\n" \
    "s_lshl_b32 s8, s8, 16\n" \
    "s_sub_u32 s62, s8, s9\n" \
    "v_lshrrev_b32 v120, 3, v255\n" \
    "s_add_u32 m0, s72, 6144\n" \
    "s_nop 0\n" \
    "buffer_load_dwordx4 v124, s[12:15], s43 offen lds\n" \
    "v_and_b32 v121, 7, v255\n" \
    "v_lshrrev_b32 v122, 2, v255\n" \
    "v_and_b32 v122, 4, v122\n" \
    "v_xor_b32 v121, v121, v122\n" \
    "v_lshlrev_b32 v120, 10, v120\n" \
    "v_lshl_add_u32 v120, v121, 4, v120\n" \
    "s_lshl_b32 s8, s5, 7\n" \
    "v_add_u32 v248, s8, v120\n" \
    "v_add_u32 v249, 7168, v248\n" \
    "v_add_u32 v250, 14336, v248\n" \
    "s_add_u32 m0, s33, 24576\n" \
    "s_add_u32 s30, s30, 16\n" \
    "s_and_b32 s30, s30, 0x70\n" \
    "s_mul_i32 s53, s30, 14336\n" \
    "s_lshl3_add_u32 s45, s30, s31\n" \
    "buffer_load_dwordx4 v226, s[16:19], s53 offen lds\n" \
    "v_add_u32 v251, 21504, v248\n" \
    "s_mul_i32 s8, s4, 229376\n" \
    "s_mul_i32 s9, s36, 14336\n" \
    "s_add_u32 s8, s8, s9\n" \
    "v_lshl_add_u32 v252, v255, 4, s8\n" \
    "v_add_u32 v253, 114688, v252\n" \
    "v_add_u32 v227, 57344, v252\n" \
    "v_add_u32 v237, 172032, v252\n" \
    "v_bfe_u32 v120, v255, 2, 2\n" \
    "v_lshrrev_b32 v121, 5, v255\n" \
    "s_add_u32 m0, s72, 7168\n" \
    "s_nop 0\n" \
    "buffer_load_dwordx4 v125, s[12:15], s43 offen lds\n" \
    "v_lshl_add_u32 v121, v121, 2, v120\n" \
    "v_lshlrev_b32 v121, 7, v121\n" \
    "v_bfe_u32 v122, v255, 4, 1\n" \
    "v_bfe_u32 v123, v255, 1, 1\n" \
    "v_lshl_add_u32 v122, v122, 1, v123\n" \
    "v_and_b32 v120, 2, v120\n" \
    "v_lshlrev_b32 v120, 1, v120\n" \
    "v_xor_b32 v122, v122, v120\n" \
    "s_add_u32 m0, s33, 28672\n" \
    "v_lshl_add_u32 v121, v122, 4, v121\n" \
    "buffer_load_dwordx4 v254, s[16:19], s53 offen lds\n" \
    "v_and_b32 v120, 1, v255\n" \
    "v_lshl_add_u32 v121, v120, 3, v121\n" \
    "v_add_u32 v234, s71, v121\n" \
    "v_xor_b32 v235, 64, v234\n" \
    "v_add_u32 v236, s72, v121\n" \
    "s_lshl_b32 s77, s4, 5\n" \
    "s_mul_i32 s9, s36, 14\n" \
    "s_lshr_b32 s9, s9, 2\n" \
    "s_add_u32 s77, s77, s9\n" \
    "s_lshl_b32 s77, s77, 7\n" \
    "s_add_u32 m0, s72, 8192\n" \
    "s_nop 0\n" \
    "buffer_load_dwordx4 v224, s[12:15], s44 offen lds\n" \
    "s_add_u32 s77, s77, 7340032\n" \
    "s_lshl2_add_u32 s8, s75, s36\n" \
    "s_lshl4_add_u32 s9, s8, s76\n" \
    "s_lshl_b32 s9, s9, 7\n" \
    "s_add_u32 s9, s9, 7340032\n" \
    "v_lshl_add_u32 v120, v255, 4, s9\n" \
    "v_cmp_gt_u32 vcc, 8, v255\n" \
    "s_mul_i32 s8, s8, 114688\n" \
    "s_lshl_b32 s9, s76, 12\n" \
    "v_cndmask_b32 v243, -16, v120, vcc\n" \
    "s_add_u32 s8, s8, s9\n" \
    "v_lshl_add_u32 v242, v255, 4, s8\n" \
    "s_mov_b32 s74, 57344\n" \
    "s_waitcnt vmcnt(11)\n" \
    "v_mov_b32 v241, 1\n" \
    "ds_write_b32 v239, v241\n" \
    "Lp1:\n" \
    "ds_read_b32 v240, v238\n" \
    "s_sub_u32 s70, s70, 1\n" \
    "s_cbranch_scc1 Labort\n" \
    "s_waitcnt lgkmcnt(0)\n" \
    "v_cmp_gt_u32 vcc, 1, v240\n" \
    "s_cbranch_vccnz Lp1\n" \
    "s_waitcnt vmcnt(9)\n" \
    "v_mov_b32 v241, 2\n" \
    "ds_write_b32 v239, v241\n" \
    "ds_read_b128 v[0:3], v228 offset:0\n" \
    "ds_read_b64_tr_b16 v[192:193], v232 offset:0\n" \
    "ds_read_b64_tr_b16 v[194:195], v232 offset:512\n" \
    "ds_read_b64_tr_b16 v[196:197], v233 offset:0\n" \
    "ds_read_b64_tr_b16 v[198:199], v233 offset:512\n" \
    "ds_read_b128 v[4:7], v229 offset:0\n" \
    "ds_read_b64_tr_b16 v[200:201], v232 offset:2048\n" \
    "ds_read_b64_tr_b16 v[202:203], v232 offset:2560\n" \
    "ds_read_b64_tr_b16 v[204:205], v233 offset:2048\n" \
    "ds_read_b64_tr_b16 v[206:207], v233 offset:2560\n" \
    "ds_read_b128 v[8:11], v230 offset:0\n" \
    "ds_read_b64_tr_b16 v[208:209], v232 offset:4096\n" \
    "ds_read_b64_tr_b16 v[210:211], v232 offset:4608\n" \
    "ds_read_b64_tr_b16 v[212:213], v233 offset:4096\n" \
    "ds_read_b64_tr_b16 v[214:215], v233 offset:4608\n" \
    "v_mfma_f32_32x32x16_bf16 v[128:143], v[96:99], v[112:115], 0\n" \
    "v_mfma_f32_32x32x16_bf16 v[144:159], v[100:103], v[112:115], 0\n" \
    "v_mfma_f32_32x32x16_bf16 v[160:175], v[104:107], v[112:115], 0\n" \
    "v_mfma_f32_32x32x16_bf16 v[176:191], v[108:111], v[112:115], 0\n" \
    "ds_read_b32 v240, v238\n" \
    "s_waitcnt lgkmcnt(11)\n" \
    "v_mfma_f32_32x32x16_bf16 v[128:143], v[192:195], v[0:3], v[128:143]\n" \
    "ds_read_b128 v[12:15], v231 offset:0\n" \
    "ds_read_b64_tr_b16 v[216:217], v232 offset:6144\n" \
    "s_add_u32 m0, s72, 9216\n" \
    "ds_read_b64_tr_b16 v[218:219], v232 offset:6656\n" \
    "buffer_load_dwordx4 v225, s[12:15], s44 offen lds\n" \
    "v_mfma_f32_32x32x16_bf16 v[144:159], v[196:199], v[0:3], v[144:159]\n" \
    "ds_read_b64_tr_b16 v[220:221], v233 offset:6144\n" \
    "s_add_u32 m0, s72, 10240\n" \
    "ds_read_b64_tr_b16 v[222:223], v233 offset:6656\n" \
    "buffer_load_dwordx4 v124, s[12:15], s44 offen lds\n" \
    "s_waitcnt lgkmcnt(11)\n" \
    "v_mfma_f32_32x32x16_bf16 v[128:143], v[200:203], v[4:7], v[128:143]\n" \
    "s_waitcnt lgkmcnt(5)\n" \
    "v_cmp_gt_u32 vcc, 2, v240\n" \
    "s_cbranch_vccnz Ls2\n" \
    "Lb3:\n" \
    "s_waitcnt vmcnt(4)\n" \
    "s_waitcnt vmcnt(3)\n" \
    "v_mov_b32 v241, 3\n" \
    "ds_write_b32 v239, v241\n" \
    "ds_read_b128 v[16:19], v228 offset:4096\n" \
    "ds_read_b64_tr_b16 v[192:193], v232 offset:8192\n" \
    "s_add_u32 m0, s72, 11264\n" \
    "ds_read_b64_tr_b16 v[194:195], v232 offset:8704\n" \
    "buffer_load_dwordx4 v125, s[12:15], s44 offen lds\n" \
    "v_mfma_f32_32x32x16_bf16 v[144:159], v[204:207], v[4:7], v[144:159]\n" \
    "ds_read_b64_tr_b16 v[196:197], v233 offset:8192\n" \
    "s_add_u32 m0, s72, 12288\n" \
    "ds_read_b64_tr_b16 v[198:199], v233 offset:8704\n" \
    "buffer_load_dwordx4 v224, s[12:15], s45 offen lds\n" \
    "v_mfma_f32_32x32x16_bf16 v[128:143], v[208:211], v[8:11], v[128:143]\n" \
    "ds_read_b128 v[20:23], v229 offset:4096\n" \
    "ds_read_b64_tr_b16 v[200:201], v232 offset:10240\n" \
    "ds_read_b64_tr_b16 v[202:203], v232 offset:10752\n" \
    "s_add_u32 m0, s33, 32768\n" \
    "s_add_u32 s30, s30, 16\n" \
    "s_and_b32 s30, s30, 0x70\n" \
    "s_mul_i32 s54, s30, 14336\n" \
    "s_lshl3_add_u32 s46, s30, s31\n" \
    "buffer_load_dwordx4 v226, s[16:19], s54 offen lds\n" \
    "v_mfma_f32_32x32x16_bf16 v[144:159], v[212:215], v[8:11], v[144:159]\n" \
    "ds_read_b64_tr_b16 v[204:205], v233 offset:10240\n" \
    "s_add_u32 m0, s72, 13312\n" \
    "ds_read_b64_tr_b16 v[206:207], v233 offset:10752\n" \
    "buffer_load_dwordx4 v225, s[12:15], s45 offen lds\n" \
    "s_waitcnt lgkmcnt(11)\n" \
    "v_mfma_f32_32x32x16_bf16 v[128:143], v[216:219], v[12:15], v[128:143]\n" \
    "ds_read_b128 v[24:27], v230 offset:4096\n" \
    "ds_read_b64_tr_b16 v[208:209], v232 offset:12288\n" \
    "ds_read_b64_tr_b16 v[210:211], v232 offset:12800\n" \
    "s_add_u32 m0, s33, 36864\n" \
    "ds_read_b32 v240, v238\n" \
    "buffer_load_dwordx4 v254, s[16:19], s54 offen lds\n" \
    "s_mov_b32 m0, 163456\n" \
    "s_cmp_eq_u32 s34, 0\n" \
    "s_cbranch_scc0 Ld4\n" \
    "buffer_load_dword v117, s[20:23], s73 offen lds\n" \
    "Ld4:\n" \
    "v_mfma_f32_32x32x16_bf16 v[144:159], v[220:223], v[12:15], v[144:159]\n" \
    "ds_read_b64_tr_b16 v[212:213], v233 offset:12288\n" \
    "s_add_u32 m0, s72, 14336\n" \
    "ds_read_b64_tr_b16 v[214:215], v233 offset:12800\n" \
    "buffer_load_dwordx4 v124, s[12:15], s45 offen lds\n" \
    "s_waitcnt lgkmcnt(11)\n" \
    "v_mfma_f32_32x32x16_bf16 v[128:143], v[192:195], v[16:19], v[128:143]\n" \
    "ds_read_b128 v[28:31], v231 offset:4096\n" \
    "ds_read_b64_tr_b16 v[216:217], v232 offset:14336\n" \
    "ds_read_b64_tr_b16 v[218:219], v232 offset:14848\n" \
    "s_add_u32 m0, s33, 40960\n" \
    "s_add_u32 s30, s30, 16\n" \
    "s_and_b32 s30, s30, 0x70\n" \
    "s_mul_i32 s55, s30, 14336\n" \
    "s_lshl3_add_u32 s47, s30, s31\n" \
    "buffer_load_dwordx4 v226, s[16:19], s55 offen lds\n" \
    "v_mfma_f32_32x32x16_bf16 v[144:159], v[196:199], v[16:19], v[144:159]\n" \
    "ds_read_b64_tr_b16 v[220:221], v233 offset:14336\n" \
    "ds_read_b64_tr_b16 v[222:223], v233 offset:14848\n" \
    "s_waitcnt lgkmcnt(7)\n" \
    "v_cmp_gt_u32 vcc, 3, v240\n" \
    "s_add_u32 m0, s72, 15360\n" \
    "s_nop 0\n" \
    "buffer_load_dwordx4 v125, s[12:15], s45 offen lds\n" \
    "v_mfma_f32_32x32x16_bf16 v[128:143], v[200:203], v[20:23], v[128:143]\n" \
    "s_cbranch_vccnz Ls5\n" \
    "Lb6:\n" \
    "s_waitcnt vmcnt(7)\n" \
    "ds_read_b128 v[32:35], v228 offset:8192\n" \
    "ds_read_b64_tr_b16 v[192:193], v232 offset:16384\n" \
    "s_add_u32 m0, s33, 45056\n" \
    "ds_read_b64_tr_b16 v[194:195], v232 offset:16896\n" \
    "buffer_load_dwordx4 v254, s[16:19], s55 offen lds\n" \
    "v_mfma_f32_32x32x16_bf16 v[144:159], v[204:207], v[20:23], v[144:159]\n" \
    "ds_read_b64_tr_b16 v[196:197], v233 offset:16384\n" \
    "s_mov_b32 m0, s72\n" \
    "ds_read_b64_tr_b16 v[198:199], v233 offset:16896\n" \
    "buffer_load_dwordx4 v224, s[12:15], s46 offen lds\n" \
    "s_waitcnt lgkmcnt(10)\n" \
    "v_mfma_f32_32x32x16_bf16 v[128:143], v[208:211], v[24:27], v[128:143]\n" \
    "ds_read_b128 v[36:39], v229 offset:8192\n" \
    "ds_read_b64_tr_b16 v[200:201], v232 offset:18432\n" \
    "s_add_u32 m0, s72, 1024\n" \
    "ds_read_b64_tr_b16 v[202:203], v232 offset:18944\n" \
    "buffer_load_dwordx4 v225, s[12:15], s46 offen lds\n" \
    "v_mfma_f32_32x32x16_bf16 v[144:159], v[212:215], v[24:27], v[144:159]\n" \
    "ds_read_b64_tr_b16 v[204:205], v233 offset:18432\n" \
    "s_add_u32 m0, s72, 2048\n" \
    "ds_read_b64_tr_b16 v[206:207], v233 offset:18944\n" \
    "buffer_load_dwordx4 v124, s[12:15], s46 offen lds\n" \
    "s_waitcnt lgkmcnt(10)\n" \
    "v_mfma_f32_32x32x16_bf16 v[128:143], v[216:219], v[28:31], v[128:143]\n" \
    "ds_read_b128 v[40:43], v230 offset:8192\n" \
    "ds_read_b64_tr_b16 v[208:209], v232 offset:20480\n" \
    "s_add_u32 m0, s72, 3072\n" \
    "ds_read_b64_tr_b16 v[210:211], v232 offset:20992\n" \
    "buffer_load_dwordx4 v125, s[12:15], s46 offen lds\n" \
    "s_mov_b32 m0, 163456\n" \
    "s_cmp_eq_u32 s34, 1\n" \
    "s_cbranch_scc0 Ld7\n" \
    "buffer_load_dword v117, s[20:23], s73 offen lds\n" \
    "Ld7:\n" \
    "v_mfma_f32_32x32x16_bf16 v[144:159], v[220:223], v[28:31], v[144:159]\n" \
    "ds_read_b64_tr_b16 v[212:213], v233 offset:20480\n" \
    "s_add_u32 m0, s72, 4096\n" \
    "ds_read_b64_tr_b16 v[214:215], v233 offset:20992\n" \
    "buffer_load_dwordx4 v224, s[12:15], s47 offen lds\n" \
    "s_waitcnt lgkmcnt(10)\n" \
    "v_mfma_f32_32x32x16_bf16 v[128:143], v[192:195], v[32:35], v[128:143]\n" \
    "ds_read_b128 v[44:47], v231 offset:8192\n" \
    "ds_read_b64_tr_b16 v[216:217], v232 offset:22528\n" \
    "ds_read_b64_tr_b16 v[218:219], v232 offset:23040\n" \
    "v_mfma_f32_32x32x16_bf16 v[144:159], v[196:199], v[32:35], v[144:159]\n" \
    "ds_read_b64_tr_b16 v[220:221], v233 offset:22528\n" \
    "ds_read_b64_tr_b16 v[222:223], v233 offset:23040\n" \
    "s_add_u32 m0, s33, 49152\n" \
    "s_add_u32 s30, s30, 16\n" \
    "s_and_b32 s30, s30, 0x70\n" \
    "s_mul_i32 s28, s30, 14336\n" \
    "s_lshl3_add_u32 s48, s30, s31\n" \
    "buffer_load_dwordx4 v226, s[16:19], s28 offen lds\n" \
    "s_waitcnt lgkmcnt(10)\n" \
    "v_mfma_f32_32x32x16_bf16 v[128:143], v[200:203], v[36:39], v[128:143]\n" \
    "s_waitcnt vmcnt(7)\n" \
    "s_waitcnt vmcnt(6)\n" \
    "v_mov_b32 v241, 5\n" \
    "ds_write_b32 v239, v241\n" \
    "ds_read_b128 v[48:51], v228 offset:12288\n" \
    "ds_read_b64_tr_b16 v[192:193], v232 offset:24576\n" \
    "s_add_u32 m0, s72, 5120\n" \
    "ds_read_b64_tr_b16 v[194:195], v232 offset:25088\n" \
    "buffer_load_dwordx4 v225, s[12:15], s47 offen lds\n" \
    "v_mfma_f32_32x32x16_bf16 v[144:159], v[204:207], v[36:39], v[144:159]\n" \
    "ds_read_b64_tr_b16 v[196:197], v233 offset:24576\n" \
    "s_add_u32 m0, s33, 53248\n" \
    "ds_read_b64_tr_b16 v[198:199], v233 offset:25088\n" \
    "buffer_load_dwordx4 v254, s[16:19], s28 offen lds\n" \
    "s_waitcnt lgkmcnt(11)\n" \
    "v_mfma_f32_32x32x16_bf16 v[128:143], v[208:211], v[40:43], v[128:143]\n" \
    "ds_read_b128 v[52:55], v229 offset:12288\n" \
    "ds_read_b64_tr_b16 v[200:201], v232 offset:26624\n" \
    "s_add_u32 m0, s72, 6144\n" \
    "ds_read_b64_tr_b16 v[202:203], v232 offset:27136\n" \
    "buffer_load_dwordx4 v124, s[12:15], s47 offen lds\n" \
    "v_mfma_f32_32x32x16_bf16 v[144:159], v[212:215], v[40:43], v[144:159]\n" \
    "ds_read_b64_tr_b16 v[204:205], v233 offset:26624\n" \
    "ds_read_b64_tr_b16 v[206:207], v233 offset:27136\n" \
    "s_waitcnt lgkmcnt(11)\n" \
    "v_mfma_f32_32x32x16_bf16 v[128:143], v[216:219], v[44:47], v[128:143]\n" \
    "ds_read_b128 v[56:59], v230 offset:12288\n" \
    "ds_read_b64_tr_b16 v[208:209], v232 offset:28672\n" \
    "ds_read_b64_tr_b16 v[210:211], v232 offset:29184\n" \
    "ds_read_b32 v240, v238\n" \
    "s_add_u32 m0, s33, 57344\n" \
    "s_add_u32 s30, s30, 16\n" \
    "s_and_b32 s30, s30, 0x70\n" \
    "s_mul_i32 s29, s30, 14336\n" \
    "s_lshl3_add_u32 s49, s30, s31\n" \
    "buffer_load_dwordx4 v226, s[16:19], s29 offen lds\n" \
    "s_mov_b32 m0, 163456\n" \
    "s_cmp_eq_u32 s34, 2\n" \
    "s_cbranch_scc0 Ld8\n" \
    "buffer_load_dword v117, s[20:23], s73 offen lds\n" \
    "Ld8:\n" \
    "v_mfma_f32_32x32x16_bf16 v[144:159], v[220:223], v[44:47], v[144:159]\n" \
    "ds_read_b64_tr_b16 v[212:213], v233 offset:28672\n" \
    "s_add_u32 m0, s72, 7168\n" \
    "ds_read_b64_tr_b16 v[214:215], v233 offset:29184\n" \
    "buffer_load_dwordx4 v125, s[12:15], s47 offen lds\n" \
    "s_waitcnt lgkmcnt(11)\n" \
    "v_mfma_f32_32x32x16_bf16 v[128:143], v[192:195], v[48:51], v[128:143]\n" \
    "ds_read_b128 v[60:63], v231 offset:12288\n" \
    "ds_read_b64_tr_b16 v[216:217], v232 offset:30720\n" \
    "s_add_u32 m0, s33, 61440\n" \
    "ds_read_b64_tr_b16 v[218:219], v232 offset:31232\n" \
    "buffer_load_dwordx4 v254, s[16:19], s29 offen lds\n" \
    "v_mfma_f32_32x32x16_bf16 v[144:159], v[196:199], v[48:51], v[144:159]\n" \
    "ds_read_b64_tr_b16 v[220:221], v233 offset:30720\n" \
    "ds_read_b64_tr_b16 v[222:223], v233 offset:31232\n" \
    "s_waitcnt lgkmcnt(7)\n" \
    "v_cmp_gt_u32 vcc, 5, v240\n" \
    "s_add_u32 m0, s72, 8192\n" \
    "s_nop 0\n" \
    "buffer_load_dwordx4 v224, s[12:15], s48 offen lds\n" \
    "v_mfma_f32_32x32x16_bf16 v[128:143], v[200:203], v[52:55], v[128:143]\n" \
    "s_cbranch_vccnz Ls9\n" \
    "Lb10:\n" \
    "s_waitcnt vmcnt(9)\n" \
    "ds_read_b128 v[64:67], v228 offset:0\n" \
    "ds_read_b64_tr_b16 v[192:193], v232 offset:32768\n" \
    "ds_read_b64_tr_b16 v[194:195], v232 offset:33280\n" \
    "v_mfma_f32_32x32x16_bf16 v[144:159], v[204:207], v[52:55], v[144:159]\n" \
    "ds_read_b64_tr_b16 v[196:197], v233 offset:32768\n" \
    "s_add_u32 m0, s72, 9216\n" \
    "ds_read_b64_tr_b16 v[198:199], v233 offset:33280\n" \
    "buffer_load_dwordx4 v225, s[12:15], s48 offen lds\n" \
    "s_waitcnt lgkmcnt(10)\n" \
    "v_mfma_f32_32x32x16_bf16 v[128:143], v[208:211], v[56:59], v[128:143]\n" \
    "ds_read_b128 v[68:71], v229 offset:0\n" \
    "ds_read_b64_tr_b16 v[200:201], v232 offset:34816\n" \
    "s_add_u32 m0, s72, 10240\n" \
    "ds_read_b64_tr_b16 v[202:203], v232 offset:35328\n" \
    "buffer_load_dwordx4 v124, s[12:15], s48 offen lds\n" \
    "v_mfma_f32_32x32x16_bf16 v[144:159], v[212:215], v[56:59], v[144:159]\n" \
    "ds_read_b64_tr_b16 v[204:205], v233 offset:34816\n" \
    "s_add_u32 m0, s72, 11264\n" \
    "ds_read_b64_tr_b16 v[206:207], v233 offset:35328\n" \
    "buffer_load_dwordx4 v125, s[12:15], s48 offen lds\n" \
    "s_waitcnt lgkmcnt(10)\n" \
    "v_mfma_f32_32x32x16_bf16 v[128:143], v[216:219], v[60:63], v[128:143]\n" \
    "ds_read_b128 v[72:75], v230 offset:0\n" \
    "ds_read_b64_tr_b16 v[208:209], v232 offset:36864\n" \
    "ds_read_b64_tr_b16 v[210:211], v232 offset:37376\n" \
    "s_mov_b32 m0, s33\n" \
    "s_add_u32 s50, s50, 128\n" \
    "buffer_load_dwordx4 v226, s[16:19], s50 offen lds\n" \
    "s_mov_b32 m0, 163456\n" \
    "s_cmp_eq_u32 s34, 3\n" \
    "s_cbranch_scc0 Ld11\n" \
    "buffer_load_dword v117, s[20:23], s73 offen lds\n" \
    "Ld11:\n" \
    "v_mfma_f32_32x32x16_bf16 v[144:159], v[220:223], v[60:63], v[144:159]\n" \
    "ds_read_b64_tr_b16 v[212:213], v233 offset:36864\n" \
    "ds_read_b64_tr_b16 v[214:215], v233 offset:37376\n" \
    "s_waitcnt lgkmcnt(10)\n" \
    "v_mfma_f32_32x32x16_bf16 v[128:143], v[192:195], v[64:67], v[128:143]\n" \
    "ds_read_b128 v[76:79], v231 offset:0\n" \
    "ds_read_b64_tr_b16 v[216:217], v232 offset:38912\n" \
    "s_add_u32 m0, s72, 12288\n" \
    "ds_read_b64_tr_b16 v[218:219], v232 offset:39424\n" \
    "buffer_load_dwordx4 v224, s[12:15], s49 offen lds\n" \
    "v_mfma_f32_32x32x16_bf16 v[144:159], v[196:199], v[64:67], v[144:159]\n" \
    "ds_read_b64_tr_b16 v[220:221], v233 offset:38912\n" \
    "s_add_u32 m0, s72, 13312\n" \
    "ds_read_b64_tr_b16 v[222:223], v233 offset:39424\n" \
    "buffer_load_dwordx4 v225, s[12:15], s49 offen lds\n" \
    "s_waitcnt lgkmcnt(10)\n" \
    "v_mfma_f32_32x32x16_bf16 v[128:143], v[200:203], v[68:71], v[128:143]\n" \
    "s_waitcnt vmcnt(8)\n" \
    "s_waitcnt vmcnt(7)\n" \
    "v_mov_b32 v241, 7\n" \
    "ds_write_b32 v239, v241\n" \
    "ds_read_b128 v[80:83], v228 offset:4096\n" \
    "ds_read_b64_tr_b16 v[192:193], v232 offset:40960\n" \
    "s_add_u32 m0, s33, 4096\n" \
    "ds_read_b64_tr_b16 v[194:195], v232 offset:41472\n" \
    "buffer_load_dwordx4 v254, s[16:19], s50 offen lds\n" \
    "v_mfma_f32_32x32x16_bf16 v[144:159], v[204:207], v[68:71], v[144:159]\n" \
    "ds_read_b64_tr_b16 v[196:197], v233 offset:40960\n" \
    "s_add_u32 m0, s72, 14336\n" \
    "ds_read_b64_tr_b16 v[198:199], v233 offset:41472\n" \
    "buffer_load_dwordx4 v124, s[12:15], s49 offen lds\n" \
    "s_waitcnt lgkmcnt(11)\n" \
    "v_mfma_f32_32x32x16_bf16 v[128:143], v[208:211], v[72:75], v[128:143]\n" \
    "ds_read_b128 v[84:87], v229 offset:4096\n" \
    "ds_read_b64_tr_b16 v[200:201], v232 offset:43008\n" \
    "ds_read_b64_tr_b16 v[202:203], v232 offset:43520\n" \
    "v_mfma_f32_32x32x16_bf16 v[144:159], v[212:215], v[72:75], v[144:159]\n" \
    "ds_read_b64_tr_b16 v[204:205], v233 offset:43008\n" \
    "ds_read_b64_tr_b16 v[206:207], v233 offset:43520\n" \
    "s_add_u32 m0, s33, 8192\n" \
    "s_add_u32 s51, s51, 128\n" \
    "buffer_load_dwordx4 v226, s[16:19], s51 offen lds\n" \
    "s_waitcnt lgkmcnt(11)\n" \
    "v_mfma_f32_32x32x16_bf16 v[128:143], v[216:219], v[76:79], v[128:143]\n" \
    "ds_read_b128 v[88:91], v230 offset:4096\n" \
    "ds_read_b64_tr_b16 v[208:209], v232 offset:45056\n" \
    "ds_read_b64_tr_b16 v[210:211], v232 offset:45568\n" \
    "s_add_u32 m0, s72, 15360\n" \
    "ds_read_b32 v240, v238\n" \
    "buffer_load_dwordx4 v125, s[12:15], s49 offen lds\n" \
    "v_mfma_f32_32x32x16_bf16 v[144:159], v[220:223], v[76:79], v[144:159]\n" \
    "ds_read_b64_tr_b16 v[212:213], v233 offset:45056\n" \
    "s_add_u32 m0, s33, 12288\n" \
    "ds_read_b64_tr_b16 v[214:215], v233 offset:45568\n" \
    "buffer_load_dwordx4 v254, s[16:19], s51 offen lds\n" \
    "s_waitcnt lgkmcnt(11)\n" \
    "v_mfma_f32_32x32x16_bf16 v[128:143], v[192:195], v[80:83], v[128:143]\n" \
    "ds_read_b128 v[92:95], v231 offset:4096\n" \
    "ds_read_b64_tr_b16 v[216:217], v232 offset:47104\n" \
    "ds_read_b64_tr_b16 v[218:219], v232 offset:47616\n" \
    "s_add_u32 m0, s33, 16384\n" \
    "s_add_u32 s52, s52, 128\n" \
    "buffer_load_dwordx4 v226, s[16:19], s52 offen lds\n" \
    "v_mfma_f32_32x32x16_bf16 v[144:159], v[196:199], v[80:83], v[144:159]\n" \
    "ds_read_b64_tr_b16 v[220:221], v233 offset:47104\n" \
    "ds_read_b64_tr_b16 v[222:223], v233 offset:47616\n" \
    "s_waitcnt lgkmcnt(7)\n" \
    "v_cmp_gt_u32 vcc, 7, v240\n" \
    "v_mfma_f32_32x32x16_bf16 v[128:143], v[200:203], v[84:87], v[128:143]\n" \
    "s_cbranch_vccnz Ls12\n" \
    "Lb13:\n" \
    "s_waitcnt vmcnt(9)\n" \
    "ds_read_b128 v[96:99], v228 offset:8192\n" \
    "ds_read_b64_tr_b16 v[192:193], v232 offset:49152\n" \
    "s_add_u32 m0, s33, 20480\n" \
    "ds_read_b64_tr_b16 v[194:195], v232 offset:49664\n" \
    "buffer_load_dwordx4 v254, s[16:19], s52 offen lds\n" \
    "v_mfma_f32_32x32x16_bf16 v[144:159], v[204:207], v[84:87], v[144:159]\n" \
    "ds_read_b64_tr_b16 v[196:197], v233 offset:49152\n" \
    "ds_read_b64_tr_b16 v[198:199], v233 offset:49664\n" \
    "s_add_u32 m0, s33, 24576\n" \
    "s_add_u32 s53, s53, 128\n" \
    "buffer_load_dwordx4 v226, s[16:19], s53 offen lds\n" \
    "s_waitcnt lgkmcnt(10)\n" \
    "v_mfma_f32_32x32x16_bf16 v[128:143], v[208:211], v[88:91], v[128:143]\n" \
    "ds_read_b128 v[100:103], v229 offset:8192\n" \
    "ds_read_b64_tr_b16 v[200:201], v232 offset:51200\n" \
    "s_add_u32 m0, s33, 28672\n" \
    "ds_read_b64_tr_b16 v[202:203], v232 offset:51712\n" \
    "buffer_load_dwordx4 v254, s[16:19], s53 offen lds\n" \
    "v_mfma_f32_32x32x16_bf16 v[144:159], v[212:215], v[88:91], v[144:159]\n" \
    "ds_read_b64_tr_b16 v[204:205], v233 offset:51200\n" \
    "ds_read_b64_tr_b16 v[206:207], v233 offset:51712\n" \
    "s_add_u32 m0, s33, 32768\n" \
    "s_add_u32 s54, s54, 128\n" \
    "buffer_load_dwordx4 v226, s[16:19], s54 offen lds\n" \
    "s_waitcnt lgkmcnt(10)\n" \
    "v_mfma_f32_32x32x16_bf16 v[128:143], v[216:219], v[92:95], v[128:143]\n" \
    "ds_read_b128 v[104:107], v230 offset:8192\n" \
    "ds_read_b64_tr_b16 v[208:209], v232 offset:53248\n" \
    "ds_read_b64_tr_b16 v[210:211], v232 offset:53760\n" \
    "v_mfma_f32_32x32x16_bf16 v[144:159], v[220:223], v[92:95], v[144:159]\n" \
    "ds_read_b64_tr_b16 v[212:213], v233 offset:53248\n" \
    "s_add_u32 m0, s33, 36864\n" \
    "ds_read_b64_tr_b16 v[214:215], v233 offset:53760\n" \
    "buffer_load_dwordx4 v254, s[16:19], s54 offen lds\n" \
    "s_waitcnt lgkmcnt(10)\n" \
    "v_mfma_f32_32x32x16_bf16 v[128:143], v[192:195], v[96:99], v[128:143]\n" \
    "ds_read_b128 v[108:111], v231 offset:8192\n" \
    "ds_read_b64_tr_b16 v[216:217], v232 offset:55296\n" \
    "s_mov_b32 m0, s72\n" \
    "ds_read_b64_tr_b16 v[218:219], v232 offset:55808\n" \
    "buffer_load_dwordx4 v248, s[20:23], s56 offen lds\n" \
    "v_mfma_f32_32x32x16_bf16 v[144:159], v[196:199], v[96:99], v[144:159]\n" \
    "ds_read_b64_tr_b16 v[220:221], v233 offset:55296\n" \
    "ds_read_b64_tr_b16 v[222:223], v233 offset:55808\n" \
    "buffer_load_dwordx4 v249, s[20:23], s56 offen offset:1024 lds\n" \
    "s_waitcnt lgkmcnt(10)\n" \
    "v_mfma_f32_32x32x16_bf16 v[128:143], v[200:203], v[100:103], v[128:143]\n" \
    "s_waitcnt vmcnt(9)\n" \
    "s_waitcnt vmcnt(8)\n" \
    "v_mov_b32 v241, 9\n" \
    "ds_write_b32 v239, v241\n" \
    "ds_read_b128 v[112:115], v228 offset:12288\n" \
    "ds_read_b64_tr_b16 v[192:193], v232 offset:57344\n" \
    "ds_read_b64_tr_b16 v[194:195], v232 offset:57856\n" \
    "buffer_load_dwordx4 v250, s[20:23], s56 offen offset:2048 lds\n" \
    "v_mov_b32 v244, 163328\n" \
    "ds_read_b96 v[244:246], v244 offset:64\n" \
    "v_mfma_f32_32x32x16_bf16 v[144:159], v[204:207], v[100:103], v[144:159]\n" \
    "ds_read_b64_tr_b16 v[196:197], v233 offset:57344\n" \
    "ds_read_b64_tr_b16 v[198:199], v233 offset:57856\n" \
    "s_waitcnt lgkmcnt(12)\n" \
    "v_mfma_f32_32x32x16_bf16 v[128:143], v[208:211], v[104:107], v[128:143]\n" \
    "ds_read_b128 v[116:119], v229 offset:12288\n" \
    "ds_read_b64_tr_b16 v[200:201], v232 offset:59392\n" \
    "ds_read_b64_tr_b16 v[202:203], v232 offset:59904\n" \
    "buffer_load_dwordx4 v251, s[20:23], s56 offen offset:3072 lds\n" \
    "v_mfma_f32_32x32x16_bf16 v[144:159], v[212:215], v[104:107], v[144:159]\n" \
    "ds_read_b64_tr_b16 v[204:205], v233 offset:59392\n" \
    "s_add_u32 m0, s72, 4096\n" \
    "ds_read_b64_tr_b16 v[206:207], v233 offset:59904\n" \
    "buffer_load_dwordx4 v248, s[20:23], s57 offen lds\n" \
    "s_waitcnt lgkmcnt(7)\n" \
    "v_cmp_eq_u32 vcc, 0, v246\n" \
    "s_cbranch_vccnz Ls14\n" \
    "Lb15:\n" \
    "v_xor_b32 v246, 64, v236\n" \
    "v_mfma_f32_32x32x16_bf16 v[128:143], v[216:219], v[108:111], v[128:143]\n" \
    "ds_read_b128 v[120:123], v230 offset:12288\n" \
    "ds_read_b64_tr_b16 v[208:209], v232 offset:61440\n" \
    "ds_read_b64_tr_b16 v[210:211], v232 offset:61952\n" \
    "ds_read_b32 v240, v238\n" \
    "buffer_load_dwordx4 v249, s[20:23], s57 offen offset:1024 lds\n" \
    "v_mfma_f32_32x32x16_bf16 v[144:159], v[220:223], v[108:111], v[144:159]\n" \
    "ds_read_b64_tr_b16 v[212:213], v233 offset:61440\n" \
    "ds_read_b64_tr_b16 v[214:215], v233 offset:61952\n" \
    "buffer_load_dwordx4 v250, s[20:23], s57 offen offset:2048 lds\n" \
    "s_waitcnt lgkmcnt(11)\n" \
    "v_mfma_f32_32x32x16_bf16 v[128:143], v[192:195], v[112:115], v[128:143]\n" \
    "ds_read_b128 v[124:127], v231 offset:12288\n" \
    "ds_read_b64_tr_b16 v[216:217], v232 offset:63488\n" \
    "ds_read_b64_tr_b16 v[218:219], v232 offset:64000\n" \
    "v_mfma_f32_32x32x16_bf16 v[144:159], v[196:199], v[112:115], v[144:159]\n" \
    "ds_read_b64_tr_b16 v[220:221], v233 offset:63488\n" \
    "ds_read_b64_tr_b16 v[222:223], v233 offset:64000\n" \
    "s_waitcnt lgkmcnt(7)\n" \
    "v_cmp_gt_u32 vcc, 9, v240\n" \
    "buffer_load_dwordx4 v251, s[20:23], s57 offen offset:3072 lds\n" \
    "v_mfma_f32_32x32x16_bf16 v[128:143], v[200:203], v[116:119], v[128:143]\n" \
    "s_cbranch_vccnz Ls16\n" \
    "Lb17:\n" \
    "v_mov_b32 v241, 10\n" \
    "ds_write_b32 v239, v241\n" \
    "ds_read_b64_tr_b16 v[192:193], v232 offset:0\n" \
    "ds_read_b64_tr_b16 v[194:195], v232 offset:512\n" \
    "s_add_u32 m0, s33, 40960\n" \
    "s_add_u32 s55, s55, 128\n" \
    "buffer_load_dwordx4 v226, s[16:19], s55 offen lds\n" \
    "v_mfma_f32_32x32x16_bf16 v[144:159], v[204:207], v[116:119], v[144:159]\n" \
    "ds_read_b64_tr_b16 v[196:197], v233 offset:0\n" \
    "s_add_u32 m0, s33, 45056\n" \
    "ds_read_b64_tr_b16 v[198:199], v233 offset:512\n" \
    "buffer_load_dwordx4 v254, s[16:19], s55 offen lds\n" \
    "ds_read_b32 v240, v238\n" \
    "s_waitcnt lgkmcnt(11)\n" \
    "v_mfma_f32_32x32x16_bf16 v[128:143], v[208:211], v[120:123], v[128:143]\n" \
    "ds_read_b64_tr_b16 v[200:201], v232 offset:2048\n" \
    "ds_read_b64_tr_b16 v[202:203], v232 offset:2560\n" \
    "s_add_u32 m0, s33, 49152\n" \
    "s_add_u32 s28, s28, 128\n" \
    "buffer_load_dwordx4 v226, s[16:19], s28 offen lds\n" \
    "v_mfma_f32_32x32x16_bf16 v[144:159], v[212:215], v[120:123], v[144:159]\n" \
    "ds_read_b64_tr_b16 v[204:205], v233 offset:2048\n" \
    "ds_read_b64_tr_b16 v[206:207], v233 offset:2560\n" \
    "s_waitcnt lgkmcnt(10)\n" \
    "v_mfma_f32_32x32x16_bf16 v[128:143], v[216:219], v[124:127], v[128:143]\n" \
    "ds_read_b64_tr_b16 v[208:209], v232 offset:4096\n" \
    "s_add_u32 m0, s33, 53248\n" \
    "ds_read_b64_tr_b16 v[210:211], v232 offset:4608\n" \
    "buffer_load_dwordx4 v254, s[16:19], s28 offen lds\n" \
    "s_waitcnt lgkmcnt(6)\n" \
    "v_cmp_gt_u32 vcc, 10, v240\n" \
    "v_mfma_f32_32x32x16_bf16 v[144:159], v[220:223], v[124:127], v[144:159]\n" \
    "s_cbranch_vccnz Ls18\n" \
    "Lb19:\n" \
    "ds_read_b64_tr_b16 v[212:213], v233 offset:4096\n" \
    "ds_read_b64_tr_b16 v[214:215], v233 offset:4608\n" \
    "s_add_u32 m0, s33, 57344\n" \
    "s_add_u32 s29, s29, 128\n" \
    "buffer_load_dwordx4 v226, s[16:19], s29 offen lds\n" \
    "v_mfma_f32_32x32x16_bf16 v[160:175], v[192:195], v[0:3], v[160:175]\n" \
    "ds_read_b64_tr_b16 v[216:217], v232 offset:6144\n" \
    "s_add_u32 m0, s33, 61440\n" \
    "ds_read_b64_tr_b16 v[218:219], v232 offset:6656\n" \
    "buffer_load_dwordx4 v254, s[16:19], s29 offen lds\n" \
    "v_mfma_f32_32x32x16_bf16 v[176:191], v[196:199], v[0:3], v[176:191]\n" \
    "ds_read_b64_tr_b16 v[220:221], v233 offset:6144\n" \
    "s_add_u32 m0, s72, 8192\n" \
    "ds_read_b64_tr_b16 v[222:223], v233 offset:6656\n" \
    "buffer_load_dwordx4 v248, s[20:23], s58 offen lds\n" \
    "v_mul_f32_e64 v128, v128, |v128|\n" \
    "v_mul_f32_e64 v129, v129, |v129|\n" \
    "v_mul_f32_e64 v130, v130, |v130|\n" \
    "v_mul_f32_e64 v131, v131, |v131|\n" \
    "s_waitcnt lgkmcnt(8)\n" \
    "v_mfma_f32_32x32x16_bf16 v[160:175], v[200:203], v[4:7], v[160:175]\n" \
    "s_waitcnt vmcnt(17)\n" \
    "v_mov_b32 v241, 11\n" \
    "ds_write_b32 v239, v241\n" \
    "ds_read_b64_tr_b16 v[192:193], v232 offset:8192\n" \
    "ds_read_b64_tr_b16 v[194:195], v232 offset:8704\n" \
    "v_mul_f32_e64 v132, v132, |v132|\n" \
    "v_mul_f32_e64 v133, v133, |v133|\n" \
    "v_mul_f32_e64 v134, v134, |v134|\n" \
    "v_mul_f32_e64 v135, v135, |v135|\n" \
    "v_mfma_f32_32x32x16_bf16 v[176:191], v[204:207], v[4:7], v[176:191]\n" \
    "ds_read_b64_tr_b16 v[196:197], v233 offset:8192\n" \
    "ds_read_b64_tr_b16 v[198:199], v233 offset:8704\n" \
    "buffer_load_dwordx4 v249, s[20:23], s58 offen offset:1024 lds\n" \
    "v_cvt_pk_bf16_f32 v128, v128, v129\n" \
    "v_cvt_pk_bf16_f32 v129, v130, v131\n" \
    "v_cvt_pk_bf16_f32 v130, v132, v133\n" \
    "v_cvt_pk_bf16_f32 v131, v134, v135\n" \
    "s_waitcnt lgkmcnt(9)\n" \
    "v_mfma_f32_32x32x16_bf16 v[160:175], v[208:211], v[8:11], v[160:175]\n" \
    "ds_read_b64_tr_b16 v[200:201], v232 offset:10240\n" \
    "ds_read_b64_tr_b16 v[202:203], v232 offset:10752\n" \
    "buffer_load_dwordx4 v250, s[20:23], s58 offen offset:2048 lds\n" \
    "v_pk_max_i16 v128, v128, 0\n" \
    "v_pk_max_i16 v129, v129, 0\n" \
    "v_pk_max_i16 v130, v130, 0\n" \
    "v_pk_max_i16 v131, v131, 0\n" \
    "v_mfma_f32_32x32x16_bf16 v[176:191], v[212:215], v[8:11], v[176:191]\n" \
    "ds_read_b64_tr_b16 v[204:205], v233 offset:10240\n" \
    "ds_read_b64_tr_b16 v[206:207], v233 offset:10752\n" \
    "buffer_load_dwordx4 v251, s[20:23], s58 offen offset:3072 lds\n" \
    "v_mul_f32_e64 v136, v136, |v136|\n" \
    "v_mul_f32_e64 v137, v137, |v137|\n" \
    "v_mul_f32_e64 v138, v138, |v138|\n" \
    "v_mul_f32_e64 v139, v139, |v139|\n" \
    "s_waitcnt lgkmcnt(9)\n" \
    "v_mfma_f32_32x32x16_bf16 v[160:175], v[216:219], v[12:15], v[160:175]\n" \
    "ds_read_b64_tr_b16 v[208:209], v232 offset:12288\n" \
    "ds_read_b64_tr_b16 v[210:211], v232 offset:12800\n" \
    "s_add_u32 m0, s72, 12288\n" \
    "ds_read_b32 v240, v238\n" \
    "buffer_load_dwordx4 v248, s[20:23], s59 offen lds\n" \
    "v_mul_f32_e64 v140, v140, |v140|\n" \
    "v_mul_f32_e64 v141, v141, |v141|\n" \
    "v_mul_f32_e64 v142, v142, |v142|\n" \
    "v_mul_f32_e64 v143, v143, |v143|\n" \
    "v_mfma_f32_32x32x16_bf16 v[176:191], v[220:223], v[12:15], v[176:191]\n" \
    "ds_read_b64_tr_b16 v[212:213], v233 offset:12288\n" \
    "ds_read_b64_tr_b16 v[214:215], v233 offset:12800\n" \
    "v_cvt_pk_bf16_f32 v136, v136, v137\n" \
    "v_cvt_pk_bf16_f32 v137, v138, v139\n" \
    "v_cvt_pk_bf16_f32 v138, v140, v141\n" \
    "v_cvt_pk_bf16_f32 v139, v142, v143\n" \
    "s_waitcnt lgkmcnt(9)\n" \
    "v_mfma_f32_32x32x16_bf16 v[160:175], v[192:195], v[16:19], v[160:175]\n" \
    "ds_read_b64_tr_b16 v[216:217], v232 offset:14336\n" \
    "ds_read_b64_tr_b16 v[218:219], v232 offset:14848\n" \
    "buffer_load_dwordx4 v249, s[20:23], s59 offen offset:1024 lds\n" \
    "v_pk_max_i16 v136, v136, 0\n" \
    "v_pk_max_i16 v137, v137, 0\n" \
    "v_pk_max_i16 v138, v138, 0\n" \
    "v_pk_max_i16 v139, v139, 0\n" \
    "s_waitcnt vmcnt(16)\n" \
    "ds_read_b64_tr_b16 v[0:1], v236 offset:0\n" \
    "ds_read_b64_tr_b16 v[2:3], v236 offset:1024\n" \
    "ds_read_b64_tr_b16 v[4:5], v246 offset:0\n" \
    "ds_read_b64_tr_b16 v[6:7], v246 offset:1024\n" \
    "v_mfma_f32_32x32x16_bf16 v[176:191], v[196:199], v[16:19], v[176:191]\n" \
    "ds_read_b64_tr_b16 v[220:221], v233 offset:14336\n" \
    "ds_read_b64_tr_b16 v[222:223], v233 offset:14848\n" \
    "s_waitcnt lgkmcnt(10)\n" \
    "v_cmp_gt_u32 vcc, 11, v240\n" \
    "buffer_load_dwordx4 v250, s[20:23], s59 offen offset:2048 lds\n" \
    "v_mul_f32_e64 v144, v144, |v144|\n" \
    "v_mul_f32_e64 v145, v145, |v145|\n" \
    "v_mul_f32_e64 v146, v146, |v146|\n" \
    "v_mul_f32_e64 v147, v147, |v147|\n" \
    "ds_read_b64_tr_b16 v[8:9], v236 offset:2048\n" \
    "ds_read_b64_tr_b16 v[10:11], v236 offset:3072\n" \
    "ds_read_b64_tr_b16 v[12:13], v246 offset:2048\n" \
    "ds_read_b64_tr_b16 v[14:15], v246 offset:3072\n" \
    "v_mfma_f32_32x32x16_bf16 v[160:175], v[200:203], v[20:23], v[160:175]\n" \
    "s_cbranch_vccnz Ls20\n" \
    "Lb21:\n" \
    "ds_read_b64_tr_b16 v[192:193], v232 offset:16384\n" \
    "ds_read_b64_tr_b16 v[194:195], v232 offset:16896\n" \
    "buffer_load_dwordx4 v251, s[20:23], s59 offen offset:3072 lds\n" \
    "v_mul_f32_e64 v148, v148, |v148|\n" \
    "v_mul_f32_e64 v149, v149, |v149|\n" \
    "v_mul_f32_e64 v150, v150, |v150|\n" \
    "v_mul_f32_e64 v151, v151, |v151|\n" \
    "v_mfma_f32_32x32x16_bf16 v[176:191], v[204:207], v[20:23], v[176:191]\n" \
    "ds_read_b64_tr_b16 v[196:197], v233 offset:16384\n" \
    "ds_read_b64_tr_b16 v[198:199], v233 offset:16896\n" \
    "v_cvt_pk_bf16_f32 v144, v144, v145\n" \
    "v_cvt_pk_bf16_f32 v145, v146, v147\n" \
    "v_cvt_pk_bf16_f32 v146, v148, v149\n" \
    "v_cvt_pk_bf16_f32 v147, v150, v151\n" \
    "s_waitcnt lgkmcnt(15)\n" \
    "v_mfma_f32_32x32x16_bf16 v[160:175], v[208:211], v[24:27], v[160:175]\n" \
    "ds_read_b64_tr_b16 v[200:201], v232 offset:18432\n" \
    "ds_read_b64_tr_b16 v[202:203], v232 offset:18944\n" \
    "v_pk_max_i16 v144, v144, 0\n" \
    "v_pk_max_i16 v145, v145, 0\n" \
    "v_pk_max_i16 v146, v146, 0\n" \
    "v_pk_max_i16 v147, v147, 0\n" \
    "s_mov_b32 m0, s72\n" \
    "s_waitcnt lgkmcnt(6)\n" \
    "buffer_load_dwordx4 v248, s[20:23], s60 offen lds\n" \
    "v_mfma_f32_32x32x16_bf16 v[176:191], v[212:215], v[24:27], v[176:191]\n" \
    "ds_read_b64_tr_b16 v[204:205], v233 offset:18432\n" \
    "ds_read_b64_tr_b16 v[206:207], v233 offset:18944\n" \
    "v_mul_f32_e64 v152, v152, |v152|\n" \
    "v_mul_f32_e64 v153, v153, |v153|\n" \
    "v_mul_f32_e64 v154, v154, |v154|\n" \
    "v_mul_f32_e64 v155, v155, |v155|\n" \
    "buffer_load_dwordx4 v249, s[20:23], s60 offen offset:1024 lds\n" \
    "v_mfma_f32_32x32x16_bf16 v[160:175], v[216:219], v[28:31], v[160:175]\n" \
    "ds_read_b64_tr_b16 v[208:209], v232 offset:20480\n" \
    "ds_read_b64_tr_b16 v[210:211], v232 offset:20992\n" \
    "v_mul_f32_e64 v156, v156, |v156|\n" \
    "v_mul_f32_e64 v157, v157, |v157|\n" \
    "v_mul_f32_e64 v158, v158, |v158|\n" \
    "v_mul_f32_e64 v159, v159, |v159|\n" \
    "buffer_load_dwordx4 v250, s[20:23], s60 offen offset:2048 lds\n" \
    "v_mfma_f32_32x32x16_bf16 v[176:191], v[220:223], v[28:31], v[176:191]\n" \
    "ds_read_b64_tr_b16 v[212:213], v233 offset:20480\n" \
    "ds_read_b64_tr_b16 v[214:215], v233 offset:20992\n" \
    "v_cvt_pk_bf16_f32 v152, v152, v153\n" \
    "v_cvt_pk_bf16_f32 v153, v154, v155\n" \
    "v_cvt_pk_bf16_f32 v154, v156, v157\n" \
    "v_cvt_pk_bf16_f32 v155, v158, v159\n" \
    "buffer_load_dwordx4 v251, s[20:23], s60 offen offset:3072 lds\n" \
    "s_waitcnt lgkmcnt(8)\n" \
    "v_mfma_f32_32x32x16_bf16 v[160:175], v[192:195], v[32:35], v[160:175]\n" \
    "ds_read_b64_tr_b16 v[216:217], v232 offset:22528\n" \
    "ds_read_b64_tr_b16 v[218:219], v232 offset:23040\n" \
    "v_pk_max_i16 v152, v152, 0\n" \
    "v_pk_max_i16 v153, v153, 0\n" \
    "v_pk_max_i16 v154, v154, 0\n" \
    "v_pk_max_i16 v155, v155, 0\n" \
    "s_waitcnt vmcnt(18)\n" \
    "ds_read_b64_tr_b16 v[16:17], v236 offset:4096\n" \
    "ds_read_b64_tr_b16 v[18:19], v236 offset:5120\n" \
    "ds_read_b64_tr_b16 v[20:21], v246 offset:4096\n" \
    "ds_read_b64_tr_b16 v[22:23], v246 offset:5120\n" \
    "v_mfma_f32_32x32x16_bf16 v[176:191], v[196:199], v[32:35], v[176:191]\n" \
    "ds_read_b64_tr_b16 v[220:221], v233 offset:22528\n" \
    "ds_read_b64_tr_b16 v[222:223], v233 offset:23040\n" \
    "buffer_store_dwordx4 v[128:131], v242, s[24:27], 0 offen sc1\n" \
    "buffer_store_dwordx4 v[136:139], v242, s[24:27], 0 offen offset:1024 sc1\n" \
    "buffer_store_dwordx4 v[144:147], v242, s[24:27], 0 offen offset:2048 sc1\n" \
    "buffer_store_dwordx4 v[152:155], v242, s[24:27], 0 offen offset:3072 sc1\n" \
    "ds_read_b64_tr_b16 v[24:25], v236 offset:6144\n" \
    "ds_read_b64_tr_b16 v[26:27], v236 offset:7168\n" \
    "ds_read_b64_tr_b16 v[28:29], v246 offset:6144\n" \
    "ds_read_b64_tr_b16 v[30:31], v246 offset:7168\n" \
    "s_waitcnt lgkmcnt(15)\n" \
    "v_mfma_f32_32x32x16_bf16 v[160:175], v[200:203], v[36:39], v[160:175]\n" \
    "s_waitcnt vmcnt(16)\n" \
    "v_mov_b32 v241, 13\n" \
    "ds_write_b32 v239, v241\n" \
    "ds_read_b64_tr_b16 v[192:193], v232 offset:24576\n" \
    "ds_read_b64_tr_b16 v[194:195], v232 offset:25088\n" \
    "s_waitcnt lgkmcnt(3)\n" \
    "s_add_u32 m0, s72, 4096\n" \
    "s_add_u32 s56, s56, 65536\n" \
    "buffer_load_dwordx4 v248, s[20:23], s56 offen lds\n" \
    "v_mfma_f32_32x32x16_bf16 v[176:191], v[204:207], v[36:39], v[176:191]\n" \
    "ds_read_b64_tr_b16 v[196:197], v233 offset:24576\n" \
    "ds_read_b64_tr_b16 v[198:199], v233 offset:25088\n" \
    "buffer_load_dwordx4 v249, s[20:23], s56 offen offset:1024 lds\n" \
    "v_mfma_f32_32x32x16_bf16 v[160:175], v[208:211], v[40:43], v[160:175]\n" \
    "ds_read_b64_tr_b16 v[200:201], v232 offset:26624\n" \
    "ds_read_b64_tr_b16 v[202:203], v232 offset:27136\n" \
    "buffer_load_dwordx4 v250, s[20:23], s56 offen offset:2048 lds\n" \
    "v_mfma_f32_32x32x16_bf16 v[176:191], v[212:215], v[40:43], v[176:191]\n" \
    "ds_read_b64_tr_b16 v[204:205], v233 offset:26624\n" \
    "ds_read_b64_tr_b16 v[206:207], v233 offset:27136\n" \
    "buffer_load_dwordx4 v251, s[20:23], s56 offen offset:3072 lds\n" \
    "v_mfma_f32_32x32x16_bf16 v[160:175], v[216:219], v[44:47], v[160:175]\n" \
    "ds_read_b64_tr_b16 v[208:209], v232 offset:28672\n" \
    "ds_read_b64_tr_b16 v[210:211], v232 offset:29184\n" \
    "ds_read_b32 v240, v238\n" \
    "v_mfma_f32_32x32x16_bf16 v[176:191], v[220:223], v[44:47], v[176:191]\n" \
    "ds_read_b64_tr_b16 v[212:213], v233 offset:28672\n" \
    "ds_read_b64_tr_b16 v[214:215], v233 offset:29184\n" \
    "s_waitcnt lgkmcnt(9)\n" \
    "v_mfma_f32_32x32x16_bf16 v[160:175], v[192:195], v[48:51], v[160:175]\n" \
    "ds_read_b64_tr_b16 v[216:217], v232 offset:30720\n" \
    "ds_read_b64_tr_b16 v[218:219], v232 offset:31232\n" \
    "s_waitcnt vmcnt(16)\n" \
    "ds_read_b64_tr_b16 v[32:33], v236 offset:8192\n" \
    "ds_read_b64_tr_b16 v[34:35], v236 offset:9216\n" \
    "ds_read_b64_tr_b16 v[36:37], v246 offset:8192\n" \
    "ds_read_b64_tr_b16 v[38:39], v246 offset:9216\n" \
    "v_mfma_f32_32x32x16_bf16 v[176:191], v[196:199], v[48:51], v[176:191]\n" \
    "ds_read_b64_tr_b16 v[220:221], v233 offset:30720\n" \
    "ds_read_b64_tr_b16 v[222:223], v233 offset:31232\n" \
    "s_waitcnt lgkmcnt(10)\n" \
    "v_cmp_gt_u32 vcc, 13, v240\n" \
    "ds_read_b64_tr_b16 v[40:41], v236 offset:10240\n" \
    "ds_read_b64_tr_b16 v[42:43], v236 offset:11264\n" \
    "ds_read_b64_tr_b16 v[44:45], v246 offset:10240\n" \
    "ds_read_b64_tr_b16 v[46:47], v246 offset:11264\n" \
    "v_mfma_f32_32x32x16_bf16 v[160:175], v[200:203], v[52:55], v[160:175]\n" \
    "s_cbranch_vccnz Ls22\n" \
    "Lb23:\n" \
    "v_mov_b32 v241, 14\n" \
    "ds_write_b32 v239, v241\n" \
    "ds_read_b64_tr_b16 v[192:193], v232 offset:32768\n" \
    "ds_read_b64_tr_b16 v[194:195], v232 offset:33280\n" \
    "v_mfma_f32_32x32x16_bf16 v[176:191], v[204:207], v[52:55], v[176:191]\n" \
    "ds_read_b64_tr_b16 v[196:197], v233 offset:32768\n" \
    "ds_read_b64_tr_b16 v[198:199], v233 offset:33280\n" \
    "s_waitcnt lgkmcnt(15)\n" \
    "v_mfma_f32_32x32x16_bf16 v[160:175], v[208:211], v[56:59], v[160:175]\n" \
    "ds_read_b64_tr_b16 v[200:201], v232 offset:34816\n" \
    "ds_read_b64_tr_b16 v[202:203], v232 offset:35328\n" \
    "v_mfma_f32_32x32x16_bf16 v[176:191], v[212:215], v[56:59], v[176:191]\n" \
    "ds_read_b64_tr_b16 v[204:205], v233 offset:34816\n" \
    "ds_read_b64_tr_b16 v[206:207], v233 offset:35328\n" \
    "s_waitcnt lgkmcnt(13)\n" \
    "v_mfma_f32_32x32x16_bf16 v[160:175], v[216:219], v[60:63], v[160:175]\n" \
    "ds_read_b64_tr_b16 v[208:209], v232 offset:36864\n" \
    "ds_read_b64_tr_b16 v[210:211], v232 offset:37376\n" \
    "v_and_b32 v226, 3, v255\n" \
    "v_bfe_u32 v224, v255, 2, 1\n" \
    "v_lshl_add_u32 v226, v224, 4, v226\n" \
    "v_lshlrev_b32 v226, 7, v226\n" \
    "v_mfma_f32_32x32x16_bf16 v[176:191], v[220:223], v[60:63], v[176:191]\n" \
    "ds_read_b64_tr_b16 v[212:213], v233 offset:36864\n" \
    "ds_read_b64_tr_b16 v[214:215], v233 offset:37376\n" \
    "s_waitcnt lgkmcnt(8)\n" \
    "v_mfma_f32_32x32x16_bf16 v[160:175], v[192:195], v[64:67], v[160:175]\n" \
    "ds_read_b64_tr_b16 v[216:217], v232 offset:38912\n" \
    "ds_read_b64_tr_b16 v[218:219], v232 offset:39424\n" \
    "ds_read_b32 v240, v238\n" \
    "s_waitcnt vmcnt(12)\n" \
    "ds_read_b64_tr_b16 v[48:49], v236 offset:12288\n" \
    "ds_read_b64_tr_b16 v[50:51], v236 offset:13312\n" \
    "ds_read_b64_tr_b16 v[52:53], v246 offset:12288\n" \
    "ds_read_b64_tr_b16 v[54:55], v246 offset:13312\n" \
    "v_bfe_u32 v229, v255, 1, 1\n" \
    "v_and_b32 v230, 1, v255\n" \
    "v_mfma_f32_32x32x16_bf16 v[176:191], v[196:199], v[64:67], v[176:191]\n" \
    "ds_read_b64_tr_b16 v[220:221], v233 offset:38912\n" \
    "ds_read_b64_tr_b16 v[222:223], v233 offset:39424\n" \
    "ds_read_b64_tr_b16 v[56:57], v236 offset:14336\n" \
    "ds_read_b64_tr_b16 v[58:59], v236 offset:15360\n" \
    "ds_read_b64_tr_b16 v[60:61], v246 offset:14336\n" \
    "ds_read_b64_tr_b16 v[62:63], v246 offset:15360\n" \
    "s_waitcnt vmcnt(4)\n" \
    "buffer_store_dwordx4 v[244:247], v243, s[24:27], 0 offen sc1\n" \
    "v_lshl_or_b32 v229, v230, 1, v229\n" \
    "s_waitcnt lgkmcnt(15)\n" \
    "v_mfma_f32_32x32x16_bf16 v[160:175], v[200:203], v[68:71], v[160:175]\n" \
    "ds_read_b64_tr_b16 v[192:193], v232 offset:40960\n" \
    "ds_read_b64_tr_b16 v[194:195], v232 offset:41472\n" \
    "v_and_b32 v230, 4, v255\n" \
    "v_or_b32 v229, v229, v230\n" \
    "v_mfma_f32_32x32x16_bf16 v[176:191], v[204:207], v[68:71], v[176:191]\n" \
    "ds_read_b64_tr_b16 v[196:197], v233 offset:40960\n" \
    "ds_read_b64_tr_b16 v[198:199], v233 offset:41472\n" \
    "s_waitcnt lgkmcnt(14)\n" \
    "v_cmp_gt_u32 vcc, 14, v240\n" \
    "v_lshrrev_b32 v230, 5, v255\n" \
    "v_mfma_f32_32x32x16_bf16 v[160:175], v[208:211], v[72:75], v[160:175]\n" \
    "s_cbranch_vccnz Ls24\n" \
    "Lb25:\n" \
    "ds_read_b64_tr_b16 v[200:201], v232 offset:43008\n" \
    "s_add_u32 m0, s72, 8192\n" \
    "ds_read_b64_tr_b16 v[202:203], v232 offset:43520\n" \
    "buffer_load_dwordx4 v248, s[20:23], s61 offen lds\n" \
    "v_xor_b32 v229, v229, v230\n" \
    "v_mfma_f32_32x32x16_bf16 v[176:191], v[212:215], v[72:75], v[176:191]\n" \
    "ds_read_b64_tr_b16 v[204:205], v233 offset:43008\n" \
    "ds_read_b64_tr_b16 v[206:207], v233 offset:43520\n" \
    "buffer_load_dwordx4 v249, s[20:23], s61 offen offset:1024 lds\n" \
    "v_and_b32 v230, 31, v255\n" \
    "v_lshlrev_b32 v230, 7, v230\n" \
    "s_waitcnt lgkmcnt(12)\n" \
    "v_mfma_f32_32x32x16_bf16 v[160:175], v[216:219], v[76:79], v[160:175]\n" \
    "ds_read_b64_tr_b16 v[208:209], v232 offset:45056\n" \
    "ds_read_b64_tr_b16 v[210:211], v232 offset:45568\n" \
    "buffer_load_dwordx4 v250, s[20:23], s61 offen offset:2048 lds\n" \
    "v_lshl_add_u32 v229, v229, 4, v230\n" \
    "v_mfma_f32_32x32x16_bf16 v[176:191], v[220:223], v[76:79], v[176:191]\n" \
    "ds_read_b64_tr_b16 v[212:213], v233 offset:45056\n" \
    "ds_read_b64_tr_b16 v[214:215], v233 offset:45568\n" \
    "buffer_load_dwordx4 v251, s[20:23], s61 offen offset:3072 lds\n" \
    "v_add_u32 v228, s72, v229\n" \
    "v_xor_b32 v229, 32, v228\n" \
    "s_waitcnt lgkmcnt(8)\n" \
    "v_mfma_f32_32x32x16_bf16 v[160:175], v[192:195], v[80:83], v[160:175]\n" \
    "ds_read_b64_tr_b16 v[216:217], v232 offset:47104\n" \
    "s_add_u32 m0, s72, 12288\n" \
    "ds_read_b64_tr_b16 v[218:219], v232 offset:47616\n" \
    "buffer_load_dwordx4 v248, s[20:23], s62 offen lds\n" \
    "v_xor_b32 v230, 64, v228\n" \
    "v_mfma_f32_32x32x16_bf16 v[176:191], v[196:199], v[80:83], v[176:191]\n" \
    "ds_read_b64_tr_b16 v[220:221], v233 offset:47104\n" \
    "ds_read_b64_tr_b16 v[222:223], v233 offset:47616\n" \
    "buffer_load_dwordx4 v249, s[20:23], s62 offen offset:1024 lds\n" \
    "v_xor_b32 v231, 96, v228\n" \
    "s_waitcnt lgkmcnt(8)\n" \
    "v_mfma_f32_32x32x16_bf16 v[160:175], v[200:203], v[84:87], v[160:175]\n" \
    "ds_read_b64_tr_b16 v[192:193], v232 offset:49152\n" \
    "ds_read_b64_tr_b16 v[194:195], v232 offset:49664\n" \
    "buffer_load_dwordx4 v250, s[20:23], s62 offen offset:2048 lds\n" \
    "v_mfma_f32_32x32x16_bf16 v[176:191], v[204:207], v[84:87], v[176:191]\n" \
    "ds_read_b64_tr_b16 v[196:197], v232 offset:51200\n" \
    "ds_read_b64_tr_b16 v[198:199], v232 offset:51712\n" \
    "buffer_load_dwordx4 v251, s[20:23], s62 offen offset:3072 lds\n" \
    "s_waitcnt lgkmcnt(8)\n" \
    "v_mfma_f32_32x32x16_bf16 v[160:175], v[208:211], v[88:91], v[160:175]\n" \
    "ds_read_b64_tr_b16 v[200:201], v232 offset:53248\n" \
    "ds_read_b64_tr_b16 v[202:203], v232 offset:53760\n" \
    "s_mov_b32 m0, s71\n" \
    "s_add_u32 s57, s57, 65536\n" \
    "buffer_load_dwordx4 v248, s[20:23], s57 offen lds\n" \
    "v_mfma_f32_32x32x16_bf16 v[176:191], v[212:215], v[88:91], v[176:191]\n" \
    "ds_read_b64_tr_b16 v[204:205], v232 offset:55296\n" \
    "ds_read_b64_tr_b16 v[206:207], v232 offset:55808\n" \
    "buffer_load_dwordx4 v249, s[20:23], s57 offen offset:1024 lds\n" \
    "s_waitcnt lgkmcnt(8)\n" \
    "v_mfma_f32_32x32x16_bf16 v[160:175], v[216:219], v[92:95], v[160:175]\n" \
    "v_mov_b32 v241, 17\n" \
    "ds_write_b32 v239, v241\n" \
    "ds_read_b64_tr_b16 v[208:209], v232 offset:57344\n" \
    "ds_read_b64_tr_b16 v[210:211], v232 offset:57856\n" \
    "v_mfma_f32_32x32x16_bf16 v[176:191], v[220:223], v[92:95], v[176:191]\n" \
    "ds_read_b64_tr_b16 v[212:213], v232 offset:59392\n" \
    "ds_read_b64_tr_b16 v[214:215], v232 offset:59904\n" \
    "buffer_load_dwordx4 v250, s[20:23], s57 offen offset:2048 lds\n" \
    "s_waitcnt lgkmcnt(9)\n" \
    "v_mfma_f32_32x32x16_bf16 v[160:175], v[192:195], v[96:99], v[160:175]\n" \
    "ds_read_b64_tr_b16 v[216:217], v232 offset:61440\n" \
    "ds_read_b64_tr_b16 v[218:219], v232 offset:61952\n" \
    "ds_read_b64_tr_b16 v[64:65], v236 offset:0\n" \
    "ds_read_b64_tr_b16 v[66:67], v236 offset:1024\n" \
    "ds_read_b64_tr_b16 v[68:69], v246 offset:0\n" \
    "ds_read_b64_tr_b16 v[70:71], v246 offset:1024\n" \
    "buffer_load_dwordx4 v251, s[20:23], s57 offen offset:3072 lds\n" \
    "v_mfma_f32_32x32x16_bf16 v[160:175], v[196:199], v[100:103], v[160:175]\n" \
    "ds_read_b64_tr_b16 v[220:221], v232 offset:63488\n" \
    "ds_read_b64_tr_b16 v[222:223], v232 offset:64000\n" \
    "ds_read_b64_tr_b16 v[72:73], v236 offset:2048\n" \
    "ds_read_b64_tr_b16 v[74:75], v236 offset:3072\n" \
    "ds_read_b64_tr_b16 v[76:77], v246 offset:2048\n" \
    "ds_read_b64_tr_b16 v[78:79], v246 offset:3072\n" \
    "s_waitcnt lgkmcnt(15)\n" \
    "v_mfma_f32_32x32x16_bf16 v[160:175], v[200:203], v[104:107], v[160:175]\n" \
    "ds_read_b64_tr_b16 v[192:193], v233 offset:49152\n" \
    "ds_read_b64_tr_b16 v[194:195], v233 offset:49664\n" \
    "s_add_u32 m0, s71, 16384\n" \
    "s_add_u32 s58, s58, 65536\n" \
    "buffer_load_dwordx4 v248, s[20:23], s58 offen lds\n" \
    "v_mfma_f32_32x32x16_bf16 v[160:175], v[204:207], v[108:111], v[160:175]\n" \
    "ds_read_b64_tr_b16 v[196:197], v233 offset:51200\n" \
    "ds_read_b64_tr_b16 v[198:199], v233 offset:51712\n" \
    "buffer_load_dwordx4 v249, s[20:23], s58 offen offset:1024 lds\n" \
    "s_waitcnt lgkmcnt(15)\n" \
    "v_mfma_f32_32x32x16_bf16 v[160:175], v[208:211], v[112:115], v[160:175]\n" \
    "ds_read_b64_tr_b16 v[200:201], v233 offset:53248\n" \
    "ds_read_b64_tr_b16 v[202:203], v233 offset:53760\n" \
    "v_mfma_f32_32x32x16_bf16 v[160:175], v[212:215], v[116:119], v[160:175]\n" \
    "ds_read_b64_tr_b16 v[204:205], v233 offset:55296\n" \
    "ds_read_b64_tr_b16 v[206:207], v233 offset:55808\n" \
    "buffer_load_dwordx4 v250, s[20:23], s58 offen offset:2048 lds\n" \
    "s_waitcnt lgkmcnt(12)\n" \
    "v_mfma_f32_32x32x16_bf16 v[160:175], v[216:219], v[120:123], v[160:175]\n" \
    "ds_read_b64_tr_b16 v[208:209], v233 offset:57344\n" \
    "ds_read_b64_tr_b16 v[210:211], v233 offset:57856\n" \
    "buffer_load_dwordx4 v251, s[20:23], s58 offen offset:3072 lds\n" \
    "v_mfma_f32_32x32x16_bf16 v[160:175], v[220:223], v[124:127], v[160:175]\n" \
    "ds_read_b64_tr_b16 v[212:213], v233 offset:59392\n" \
    "ds_read_b64_tr_b16 v[214:215], v233 offset:59904\n" \
    "s_waitcnt lgkmcnt(8)\n" \
    "v_mfma_f32_32x32x16_bf16 v[176:191], v[192:195], v[96:99], v[176:191]\n" \
    "ds_read_b64_tr_b16 v[216:217], v233 offset:61440\n" \
    "ds_read_b64_tr_b16 v[218:219], v233 offset:61952\n" \
    "v_mfma_f32_32x32x16_bf16 v[176:191], v[196:199], v[100:103], v[176:191]\n" \
    "ds_read_b64_tr_b16 v[220:221], v233 offset:63488\n" \
    "ds_read_b64_tr_b16 v[222:223], v233 offset:64000\n" \
    "s_nop 2\n" \
    "v_mul_f32_e64 v160, v160, |v160|\n" \
    "v_mul_f32_e64 v161, v161, |v161|\n" \
    "v_mul_f32_e64 v162, v162, |v162|\n" \
    "v_mul_f32_e64 v163, v163, |v163|\n" \
    "v_mul_f32_e64 v164, v164, |v164|\n" \
    "s_waitcnt lgkmcnt(8)\n" \
    "v_mfma_f32_32x32x16_bf16 v[176:191], v[200:203], v[104:107], v[176:191]\n" \
    "v_mul_f32_e64 v165, v165, |v165|\n" \
    "v_mul_f32_e64 v166, v166, |v166|\n" \
    "v_mul_f32_e64 v167, v167, |v167|\n" \
    "v_cvt_pk_bf16_f32 v160, v160, v161\n" \
    "v_cvt_pk_bf16_f32 v161, v162, v163\n" \
    "v_mfma_f32_32x32x16_bf16 v[176:191], v[204:207], v[108:111], v[176:191]\n" \
    "v_cvt_pk_bf16_f32 v162, v164, v165\n" \
    "v_cvt_pk_bf16_f32 v163, v166, v167\n" \
    "v_pk_max_i16 v160, v160, 0\n" \
    "v_pk_max_i16 v161, v161, 0\n" \
    "s_waitcnt lgkmcnt(4)\n" \
    "v_mfma_f32_32x32x16_bf16 v[176:191], v[208:211], v[112:115], v[176:191]\n" \
    "v_pk_max_i16 v162, v162, 0\n" \
    "v_pk_max_i16 v163, v163, 0\n" \
    "v_mul_f32_e64 v168, v168, |v168|\n" \
    "v_mul_f32_e64 v169, v169, |v169|\n" \
    "v_mul_f32_e64 v170, v170, |v170|\n" \
    "v_mfma_f32_32x32x16_bf16 v[176:191], v[212:215], v[116:119], v[176:191]\n" \
    "buffer_load_dwordx2 v[224:225], v226, s[24:27], s77 offen sc1\n" \
    "v_mul_f32_e64 v171, v171, |v171|\n" \
    "v_mul_f32_e64 v172, v172, |v172|\n" \
    "v_mul_f32_e64 v173, v173, |v173|\n" \
    "v_mul_f32_e64 v174, v174, |v174|\n" \
    "s_waitcnt lgkmcnt(0)\n" \
    "v_mfma_f32_32x32x16_bf16 v[176:191], v[216:219], v[120:123], v[176:191]\n" \
    "v_mul_f32_e64 v175, v175, |v175|\n" \
    "v_cvt_pk_bf16_f32 v168, v168, v169\n" \
    "v_cvt_pk_bf16_f32 v169, v170, v171\n" \
    "v_cvt_pk_bf16_f32 v170, v172, v173\n" \
    "v_cvt_pk_bf16_f32 v171, v174, v175\n" \
    "v_mfma_f32_32x32x16_bf16 v[176:191], v[220:223], v[124:127], v[176:191]\n" \
    "v_mov_b32 v241, 18\n" \
    "ds_write_b32 v239, v241\n" \
    "v_pk_max_i16 v168, v168, 0\n" \
    "v_pk_max_i16 v169, v169, 0\n" \
    "v_pk_max_i16 v170, v170, 0\n" \
    "v_pk_max_i16 v171, v171, 0\n" \
    "buffer_store_dwordx4 v[160:163], v242, s[24:27], s74 offen sc1\n" \
    "buffer_store_dwordx4 v[168:171], v242, s[24:27], s74 offen offset:1024 sc1\n" \
    "s_nop 3\n" \
    "v_mul_f32_e64 v176, v176, |v176|\n" \
    "v_mul_f32_e64 v177, v177, |v177|\n" \
    "v_mul_f32_e64 v178, v178, |v178|\n" \
    "v_mul_f32_e64 v179, v179, |v179|\n" \
    "v_mul_f32_e64 v180, v180, |v180|\n" \
    "v_mul_f32_e64 v181, v181, |v181|\n" \
    "v_mul_f32_e64 v182, v182, |v182|\n" \
    "v_mul_f32_e64 v183, v183, |v183|\n" \
    "v_cvt_pk_bf16_f32 v176, v176, v177\n" \
    "v_cvt_pk_bf16_f32 v177, v178, v179\n" \
    "v_cvt_pk_bf16_f32 v178, v180, v181\n" \
    "v_cvt_pk_bf16_f32 v179, v182, v183\n" \
    "v_pk_max_i16 v176, v176, 0\n" \
    "v_pk_max_i16 v177, v177, 0\n" \
    "v_pk_max_i16 v178, v178, 0\n" \
    "v_pk_max_i16 v179, v179, 0\n" \
    "buffer_store_dwordx4 v[176:179], v242, s[24:27], s74 offen offset:2048 sc1\n" \
    "v_mul_f32_e64 v184, v184, |v184|\n" \
    "v_mul_f32_e64 v185, v185, |v185|\n" \
    "v_mul_f32_e64 v186, v186, |v186|\n" \
    "v_mul_f32_e64 v187, v187, |v187|\n" \
    "v_mul_f32_e64 v188, v188, |v188|\n" \
    "v_mul_f32_e64 v189, v189, |v189|\n" \
    "v_mul_f32_e64 v190, v190, |v190|\n" \
    "v_mul_f32_e64 v191, v191, |v191|\n" \
    "v_cvt_pk_bf16_f32 v184, v184, v185\n" \
    "v_cvt_pk_bf16_f32 v185, v186, v187\n" \
    "v_cvt_pk_bf16_f32 v186, v188, v189\n" \
    "v_cvt_pk_bf16_f32 v187, v190, v191\n" \
    "v_pk_max_i16 v184, v184, 0\n" \
    "v_pk_max_i16 v185, v185, 0\n" \
    "v_pk_max_i16 v186, v186, 0\n" \
    "v_pk_max_i16 v187, v187, 0\n" \
    "buffer_store_dwordx4 v[184:187], v242, s[24:27], s74 offen offset:3072 sc1\n" \
    "Ljoin:\n" \
    "s_waitcnt vmcnt(4)\n" \
    "v_cmp_ne_u64 vcc, v[224:225], v[244:245]\n" \
    "s_cbranch_vccnz Ls26\n" \
    "Lb27:\n" \
    "s_add_u32 s77, s77, 131072\n" \
    "buffer_load_dwordx4 v[160:163], v252, s[24:27], s63 offen sc1\n" \
    "buffer_load_dwordx4 v[164:167], v252, s[24:27], s63 offen offset:1024 sc1\n" \
    "buffer_load_dwordx4 v[168:171], v252, s[24:27], s64 offen sc1\n" \
    "buffer_load_dwordx4 v[172:175], v252, s[24:27], s64 offen offset:1024 sc1\n" \
    "buffer_load_dwordx4 v[176:179], v252, s[24:27], s65 offen sc1\n" \
    "buffer_load_dwordx4 v[180:183], v252, s[24:27], s65 offen offset:1024 sc1\n" \
    "buffer_load_dwordx4 v[184:187], v252, s[24:27], s66 offen sc1\n" \
    "buffer_load_dwordx4 v[188:191], v252, s[24:27], s66 offen offset:1024 sc1\n" \
    "s_waitcnt vmcnt(8)\n" \
    "s_mov_b32 s73, 131072\n" \
    "buffer_store_dwordx4 v[244:247], v243, s[24:27], s73 offen sc1\n" \
    "s_waitcnt vmcnt(8)\n" \
    "v_mfma_f32_32x32x16_bf16 v[128:143], v[0:3], v[160:163], 0\n" \
    "v_mfma_f32_32x32x16_bf16 v[144:159], v[4:7], v[160:163], 0\n" \
    "buffer_load_dwordx4 v[160:163], v252, s[24:27], s67 offen sc1\n" \
    "ds_read_b64_tr_b16 v[80:81], v236 offset:4096\n" \
    "ds_read_b64_tr_b16 v[82:83], v236 offset:5120\n" \
    "ds_read_b64_tr_b16 v[84:85], v246 offset:4096\n" \
    "ds_read_b64_tr_b16 v[86:87], v246 offset:5120\n" \
    "ds_read_b64_tr_b16 v[88:89], v236 offset:6144\n" \
    "ds_read_b64_tr_b16 v[90:91], v236 offset:7168\n" \
    "ds_read_b64_tr_b16 v[92:93], v246 offset:6144\n" \
    "ds_read_b64_tr_b16 v[94:95], v246 offset:7168\n" \
    "ds_read_b32 v240, v238\n" \
    "s_waitcnt vmcnt(8)\n" \
    "v_mfma_f32_32x32x16_bf16 v[128:143], v[8:11], v[164:167], v[128:143]\n" \
    "v_mfma_f32_32x32x16_bf16 v[144:159], v[12:15], v[164:167], v[144:159]\n" \
    "buffer_load_dwordx4 v[164:167], v252, s[24:27], s67 offen offset:1024 sc1\n" \
    "s_waitcnt lgkmcnt(0)\n" \
    "v_cmp_gt_u32 vcc, 18, v240\n" \
    "s_cbranch_vccnz Ls28\n" \
    "Lb29:\n" \
    "s_add_u32 m0, s71, 32768\n" \
    "s_add_u32 s59, s59, 65536\n" \
    "buffer_load_dwordx4 v248, s[20:23], s59 offen lds\n" \
    "buffer_load_dwordx4 v249, s[20:23], s59 offen offset:1024 lds\n" \
    "s_waitcnt vmcnt(10)\n" \
    "v_mfma_f32_32x32x16_bf16 v[128:143], v[16:19], v[168:171], v[128:143]\n" \
    "v_mfma_f32_32x32x16_bf16 v[144:159], v[20:23], v[168:171], v[144:159]\n" \
    "buffer_load_dwordx4 v[168:171], v252, s[24:27], s68 offen sc1\n" \
    "buffer_load_dwordx4 v250, s[20:23], s59 offen offset:2048 lds\n" \
    "buffer_load_dwordx4 v251, s[20:23], s59 offen offset:3072 lds\n" \
    "s_waitcnt vmcnt(12)\n" \
    "v_mfma_f32_32x32x16_bf16 v[128:143], v[24:27], v[172:175], v[128:143]\n" \
    "v_mfma_f32_32x32x16_bf16 v[144:159], v[28:31], v[172:175], v[144:159]\n" \
    "buffer_load_dwordx4 v[172:175], v252, s[24:27], s68 offen offset:1024 sc1\n" \
    "s_add_u32 m0, s71, 49152\n" \
    "s_add_u32 s60, s60, 65536\n" \
    "buffer_load_dwordx4 v248, s[20:23], s60 offen lds\n" \
    "s_waitcnt vmcnt(13)\n" \
    "v_mfma_f32_32x32x16_bf16 v[128:143], v[32:35], v[176:179], v[128:143]\n" \
    "v_mfma_f32_32x32x16_bf16 v[144:159], v[36:39], v[176:179], v[144:159]\n" \
    "buffer_load_dwordx4 v[176:179], v252, s[24:27], s69 offen sc1\n" \
    "buffer_load_dwordx4 v249, s[20:23], s60 offen offset:1024 lds\n" \
    "s_waitcnt vmcnt(14)\n" \
    "v_mfma_f32_32x32x16_bf16 v[128:143], v[40:43], v[180:183], v[128:143]\n" \
    "v_mfma_f32_32x32x16_bf16 v[144:159], v[44:47], v[180:183], v[144:159]\n" \
    "buffer_load_dwordx4 v[180:183], v252, s[24:27], s69 offen offset:1024 sc1\n" \
    "buffer_load_dwordx4 v250, s[20:23], s60 offen offset:2048 lds\n" \
    "s_waitcnt vmcnt(15)\n" \
    "v_mfma_f32_32x32x16_bf16 v[128:143], v[48:51], v[184:187], v[128:143]\n" \
    "v_mfma_f32_32x32x16_bf16 v[144:159], v[52:55], v[184:187], v[144:159]\n" \
    "buffer_load_dwordx4 v[184:187], v253, s[24:27], s63 offen sc1\n" \
    "buffer_load_dwordx4 v251, s[20:23], s60 offen offset:3072 lds\n" \
    "s_waitcnt vmcnt(16)\n" \
    "v_mfma_f32_32x32x16_bf16 v[128:143], v[56:59], v[188:191], v[128:143]\n" \
    "ds_read_b64_tr_b16 v[192:193], v236 offset:8192\n" \
    "ds_read_b64_tr_b16 v[194:195], v236 offset:9216\n" \
    "ds_read_b64_tr_b16 v[196:197], v246 offset:8192\n" \
    "ds_read_b64_tr_b16 v[198:199], v246 offset:9216\n" \
    "v_mfma_f32_32x32x16_bf16 v[144:159], v[60:63], v[188:191], v[144:159]\n" \
    "buffer_load_dwordx4 v[188:191], v253, s[24:27], s63 offen offset:1024 sc1\n" \
    "s_mov_b32 m0, s72\n" \
    "s_add_u32 s61, s61, 65536\n" \
    "buffer_load_dwordx4 v248, s[20:23], s61 offen lds\n" \
    "s_waitcnt vmcnt(16)\n" \
    "v_mfma_f32_32x32x16_bf16 v[128:143], v[64:67], v[160:163], v[128:143]\n" \
    "ds_read_b64_tr_b16 v[200:201], v236 offset:10240\n" \
    "ds_read_b64_tr_b16 v[202:203], v236 offset:11264\n" \
    "ds_read_b64_tr_b16 v[204:205], v246 offset:10240\n" \
    "ds_read_b64_tr_b16 v[206:207], v246 offset:11264\n" \
    "v_mfma_f32_32x32x16_bf16 v[144:159], v[68:71], v[160:163], v[144:159]\n" \
    "buffer_load_dwordx4 v[160:163], v253, s[24:27], s64 offen sc1\n" \
    "buffer_load_dwordx4 v249, s[20:23], s61 offen offset:1024 lds\n" \
    "s_waitcnt vmcnt(17)\n" \
    "v_mfma_f32_32x32x16_bf16 v[128:143], v[72:75], v[164:167], v[128:143]\n" \
    "ds_read_b64_tr_b16 v[208:209], v236 offset:12288\n" \
    "ds_read_b64_tr_b16 v[210:211], v236 offset:13312\n" \
    "ds_read_b64_tr_b16 v[212:213], v246 offset:12288\n" \
    "ds_read_b64_tr_b16 v[214:215], v246 offset:13312\n" \
    "v_mfma_f32_32x32x16_bf16 v[144:159], v[76:79], v[164:167], v[144:159]\n" \
    "buffer_load_dwordx4 v[164:167], v253, s[24:27], s64 offen offset:1024 sc1\n" \
    "buffer_load_dwordx4 v250, s[20:23], s61 offen offset:2048 lds\n" \
    "s_waitcnt vmcnt(16)\n" \
    "s_waitcnt lgkmcnt(8)\n" \
    "v_mfma_f32_32x32x16_bf16 v[128:143], v[192:195], v[168:171], v[128:143]\n" \
    "ds_read_b64_tr_b16 v[216:217], v236 offset:14336\n" \
    "ds_read_b64_tr_b16 v[218:219], v236 offset:15360\n" \
    "ds_read_b64_tr_b16 v[220:221], v246 offset:14336\n" \
    "ds_read_b64_tr_b16 v[222:223], v246 offset:15360\n" \
    "v_mfma_f32_32x32x16_bf16 v[144:159], v[196:199], v[168:171], v[144:159]\n" \
    "buffer_load_dwordx4 v[168:171], v253, s[24:27], s65 offen sc1\n" \
    "buffer_load_dwordx4 v251, s[20:23], s61 offen offset:3072 lds\n" \
    "s_waitcnt vmcnt(15)\n" \
    "s_waitcnt lgkmcnt(8)\n" \
    "v_mfma_f32_32x32x16_bf16 v[128:143], v[200:203], v[172:175], v[128:143]\n" \
    "v_mfma_f32_32x32x16_bf16 v[144:159], v[204:207], v[172:175], v[144:159]\n" \
    "buffer_load_dwordx4 v[172:175], v253, s[24:27], s65 offen offset:1024 sc1\n" \
    "s_add_u32 m0, s72, 4096\n" \
    "s_add_u32 s62, s62, 65536\n" \
    "buffer_load_dwordx4 v248, s[20:23], s62 offen lds\n" \
    "s_waitcnt vmcnt(15)\n" \
    "s_waitcnt lgkmcnt(4)\n" \
    "v_mfma_f32_32x32x16_bf16 v[128:143], v[208:211], v[176:179], v[128:143]\n" \
    "v_mfma_f32_32x32x16_bf16 v[144:159], v[212:215], v[176:179], v[144:159]\n" \
    "buffer_load_dwordx4 v[176:179], v253, s[24:27], s66 offen sc1\n" \
    "buffer_load_dwordx4 v249, s[20:23], s62 offen offset:1024 lds\n" \
    "s_waitcnt vmcnt(15)\n" \
    "s_waitcnt lgkmcnt(0)\n" \
    "v_mfma_f32_32x32x16_bf16 v[128:143], v[216:219], v[180:183], v[128:143]\n" \
    "v_mfma_f32_32x32x16_bf16 v[144:159], v[220:223], v[180:183], v[144:159]\n" \
    "buffer_load_dwordx4 v[180:183], v253, s[24:27], s66 offen offset:1024 sc1\n" \
    "buffer_load_dwordx2 v[224:225], v226, s[24:27], s77 offen sc1\n" \
    "buffer_load_dwordx4 v250, s[20:23], s62 offen offset:2048 lds\n" \
    "s_waitcnt vmcnt(16)\n" \
    "v_mfma_f32_32x32x16_bf16 v[96:111], v[0:3], v[184:187], 0\n" \
    "v_mfma_f32_32x32x16_bf16 v[112:127], v[4:7], v[184:187], 0\n" \
    "buffer_load_dwordx4 v[184:187], v253, s[24:27], s67 offen sc1\n" \
    "buffer_load_dwordx4 v[0:3], v253, s[24:27], s67 offen offset:1024 sc1\n" \
    "buffer_load_dwordx4 v251, s[20:23], s62 offen offset:3072 lds\n" \
    "s_waitcnt vmcnt(17)\n" \
    "v_mfma_f32_32x32x16_bf16 v[96:111], v[8:11], v[188:191], v[96:111]\n" \
    "v_mfma_f32_32x32x16_bf16 v[112:127], v[12:15], v[188:191], v[112:127]\n" \
    "buffer_load_dwordx4 v[4:7], v253, s[24:27], s68 offen sc1\n" \
    "buffer_load_dwordx4 v[188:191], v253, s[24:27], s68 offen offset:1024 sc1\n" \
    "s_waitcnt vmcnt(17)\n" \
    "v_mfma_f32_32x32x16_bf16 v[96:111], v[16:19], v[160:163], v[96:111]\n" \
    "v_mfma_f32_32x32x16_bf16 v[112:127], v[20:23], v[160:163], v[112:127]\n" \
    "buffer_load_dwordx4 v[8:11], v253, s[24:27], s69 offen sc1\n" \
    "s_waitcnt vmcnt(16)\n" \
    "v_mfma_f32_32x32x16_bf16 v[96:111], v[24:27], v[164:167], v[96:111]\n" \
    "v_mfma_f32_32x32x16_bf16 v[112:127], v[28:31], v[164:167], v[112:127]\n" \
    "buffer_load_dwordx4 v[12:15], v253, s[24:27], s69 offen offset:1024 sc1\n" \
    "s_waitcnt vmcnt(15)\n" \
    "v_mfma_f32_32x32x16_bf16 v[96:111], v[32:35], v[168:171], v[96:111]\n" \
    "v_mfma_f32_32x32x16_bf16 v[112:127], v[36:39], v[168:171], v[112:127]\n" \
    "s_waitcnt vmcnt(13)\n" \
    "v_mfma_f32_32x32x16_bf16 v[96:111], v[40:43], v[172:175], v[96:111]\n" \
    "v_mfma_f32_32x32x16_bf16 v[112:127], v[44:47], v[172:175], v[112:127]\n" \
    "s_waitcnt vmcnt(11)\n" \
    "v_mfma_f32_32x32x16_bf16 v[96:111], v[48:51], v[176:179], v[96:111]\n" \
    "v_mfma_f32_32x32x16_bf16 v[112:127], v[52:55], v[176:179], v[112:127]\n" \
    "s_waitcnt vmcnt(8)\n" \
    "v_cmp_ne_u64 vcc, v[224:225], v[244:245]\n" \
    "s_cbranch_vccnz Ls30\n" \
    "Lb31:\n" \
    "buffer_load_dwordx4 v[160:163], v227, s[24:27], s63 offen sc1\n" \
    "buffer_load_dwordx4 v[16:19], v227, s[24:27], s63 offen offset:1024 sc1\n" \
    "v_mfma_f32_32x32x16_bf16 v[96:111], v[56:59], v[180:183], v[96:111]\n" \
    "ds_read_b64_tr_b16 v[192:193], v236 offset:8192\n" \
    "ds_read_b64_tr_b16 v[194:195], v236 offset:9216\n" \
    "ds_read_b64_tr_b16 v[196:197], v246 offset:8192\n" \
    "ds_read_b64_tr_b16 v[198:199], v246 offset:9216\n" \
    "v_mfma_f32_32x32x16_bf16 v[112:127], v[60:63], v[180:183], v[112:127]\n" \
    "buffer_load_dwordx4 v[20:23], v227, s[24:27], s64 offen sc1\n" \
    "buffer_load_dwordx4 v[164:167], v227, s[24:27], s64 offen offset:1024 sc1\n" \
    "s_waitcnt vmcnt(10)\n" \
    "v_mfma_f32_32x32x16_bf16 v[96:111], v[64:67], v[184:187], v[96:111]\n" \
    "ds_read_b64_tr_b16 v[200:201], v236 offset:10240\n" \
    "ds_read_b64_tr_b16 v[202:203], v236 offset:11264\n" \
    "ds_read_b64_tr_b16 v[204:205], v246 offset:10240\n" \
    "ds_read_b64_tr_b16 v[206:207], v246 offset:11264\n" \
    "v_mfma_f32_32x32x16_bf16 v[112:127], v[68:71], v[184:187], v[112:127]\n" \
    "buffer_load_dwordx4 v[24:27], v227, s[24:27], s65 offen sc1\n" \
    "s_waitcnt vmcnt(10)\n" \
    "v_mfma_f32_32x32x16_bf16 v[96:111], v[72:75], v[0:3], v[96:111]\n" \
    "ds_read_b64_tr_b16 v[208:209], v236 offset:12288\n" \
    "ds_read_b64_tr_b16 v[210:211], v236 offset:13312\n" \
    "ds_read_b64_tr_b16 v[212:213], v246 offset:12288\n" \
    "ds_read_b64_tr_b16 v[214:215], v246 offset:13312\n" \
    "v_mfma_f32_32x32x16_bf16 v[112:127], v[76:79], v[0:3], v[112:127]\n" \
    "buffer_load_dwordx4 v[28:31], v227, s[24:27], s65 offen offset:1024 sc1\n" \
    "s_waitcnt vmcnt(9)\n" \
    "s_waitcnt lgkmcnt(8)\n" \
    "v_mfma_f32_32x32x16_bf16 v[96:111], v[192:195], v[4:7], v[96:111]\n" \
    "ds_read_b64_tr_b16 v[216:217], v236 offset:14336\n" \
    "ds_read_b64_tr_b16 v[218:219], v236 offset:15360\n" \
    "ds_read_b64_tr_b16 v[220:221], v246 offset:14336\n" \
    "ds_read_b64_tr_b16 v[222:223], v246 offset:15360\n" \
    "v_mfma_f32_32x32x16_bf16 v[112:127], v[196:199], v[4:7], v[112:127]\n" \
    "buffer_load_dwordx4 v[168:171], v227, s[24:27], s66 offen sc1\n" \
    "s_waitcnt vmcnt(9)\n" \
    "s_waitcnt lgkmcnt(8)\n" \
    "v_mfma_f32_32x32x16_bf16 v[96:111], v[200:203], v[188:191], v[96:111]\n" \
    "v_mfma_f32_32x32x16_bf16 v[112:127], v[204:207], v[188:191], v[112:127]\n" \
    "buffer_load_dwordx4 v[32:35], v227, s[24:27], s66 offen offset:1024 sc1\n" \
    "s_waitcnt vmcnt(9)\n" \
    "s_waitcnt lgkmcnt(4)\n" \
    "v_mfma_f32_32x32x16_bf16 v[96:111], v[208:211], v[8:11], v[96:111]\n" \
    "v_mfma_f32_32x32x16_bf16 v[112:127], v[212:215], v[8:11], v[112:127]\n" \
    "buffer_load_dwordx4 v[36:39], v227, s[24:27], s67 offen sc1\n" \
    "s_waitcnt vmcnt(9)\n" \
    "s_waitcnt lgkmcnt(0)\n" \
    "v_mfma_f32_32x32x16_bf16 v[96:111], v[216:219], v[12:15], v[96:111]\n" \
    "ds_read_b64_tr_b16 v[192:193], v234 offset:0\n" \
    "ds_read_b64_tr_b16 v[194:195], v234 offset:1024\n" \
    "ds_read_b64_tr_b16 v[196:197], v235 offset:0\n" \
    "ds_read_b64_tr_b16 v[198:199], v235 offset:1024\n" \
    "v_mfma_f32_32x32x16_bf16 v[112:127], v[220:223], v[12:15], v[112:127]\n" \
    "buffer_load_dwordx4 v[172:175], v227, s[24:27], s67 offen offset:1024 sc1\n" \
    "s_waitcnt vmcnt(9)\n" \
    "v_mfma_f32_32x32x16_bf16 v[128:143], v[80:83], v[160:163], v[128:143]\n" \
    "ds_read_b64_tr_b16 v[200:201], v234 offset:2048\n" \
    "ds_read_b64_tr_b16 v[202:203], v234 offset:3072\n" \
    "ds_read_b64_tr_b16 v[204:205], v235 offset:2048\n" \
    "ds_read_b64_tr_b16 v[206:207], v235 offset:3072\n" \
    "v_mfma_f32_32x32x16_bf16 v[144:159], v[84:87], v[160:163], v[144:159]\n" \
    "buffer_load_dwordx4 v[40:43], v227, s[24:27], s68 offen sc1\n" \
    "s_waitcnt vmcnt(9)\n" \
    "v_mfma_f32_32x32x16_bf16 v[128:143], v[88:91], v[16:19], v[128:143]\n" \
    "ds_read_b64_tr_b16 v[208:209], v234 offset:16384\n" \
    "ds_read_b64_tr_b16 v[210:211], v234 offset:17408\n" \
    "ds_read_b64_tr_b16 v[212:213], v235 offset:16384\n" \
    "ds_read_b64_tr_b16 v[214:215], v235 offset:17408\n" \
    "v_mfma_f32_32x32x16_bf16 v[144:159], v[92:95], v[16:19], v[144:159]\n" \
    "buffer_load_dwordx4 v[44:47], v227, s[24:27], s68 offen offset:1024 sc1\n" \
    "s_waitcnt vmcnt(9)\n" \
    "s_waitcnt lgkmcnt(8)\n" \
    "v_mfma_f32_32x32x16_bf16 v[128:143], v[192:195], v[20:23], v[128:143]\n" \
    "ds_read_b64_tr_b16 v[216:217], v234 offset:18432\n" \
    "ds_read_b64_tr_b16 v[218:219], v234 offset:19456\n" \
    "ds_read_b64_tr_b16 v[220:221], v235 offset:18432\n" \
    "ds_read_b64_tr_b16 v[222:223], v235 offset:19456\n" \
    "v_mfma_f32_32x32x16_bf16 v[144:159], v[196:199], v[20:23], v[144:159]\n" \
    "buffer_load_dwordx4 v[176:179], v227, s[24:27], s69 offen sc1\n" \
    "s_waitcnt vmcnt(9)\n" \
    "s_waitcnt lgkmcnt(8)\n" \
    "v_mfma_f32_32x32x16_bf16 v[128:143], v[200:203], v[164:167], v[128:143]\n" \
    "ds_read_b64_tr_b16 v[192:193], v234 offset:32768\n" \
    "ds_read_b64_tr_b16 v[194:195], v234 offset:33792\n" \
    "ds_read_b64_tr_b16 v[196:197], v235 offset:32768\n" \
    "ds_read_b64_tr_b16 v[198:199], v235 offset:33792\n" \
    "v_mfma_f32_32x32x16_bf16 v[144:159], v[204:207], v[164:167], v[144:159]\n" \
    "buffer_load_dwordx4 v[48:51], v227, s[24:27], s69 offen offset:1024 sc1\n" \
    "s_waitcnt vmcnt(9)\n" \
    "s_waitcnt lgkmcnt(8)\n" \
    "v_mfma_f32_32x32x16_bf16 v[128:143], v[208:211], v[24:27], v[128:143]\n" \
    "ds_read_b64_tr_b16 v[200:201], v234 offset:34816\n" \
    "ds_read_b64_tr_b16 v[202:203], v234 offset:35840\n" \
    "ds_read_b64_tr_b16 v[204:205], v235 offset:34816\n" \
    "ds_read_b64_tr_b16 v[206:207], v235 offset:35840\n" \
    "v_mfma_f32_32x32x16_bf16 v[144:159], v[212:215], v[24:27], v[144:159]\n" \
    "buffer_load_dwordx4 v[52:55], v237, s[24:27], s63 offen sc1\n" \
    "v_mov_b32 v241, 20\n" \
    "ds_write_b32 v239, v241\n" \
    "s_waitcnt vmcnt(9)\n" \
    "s_waitcnt lgkmcnt(9)\n" \
    "v_mfma_f32_32x32x16_bf16 v[128:143], v[216:219], v[28:31], v[128:143]\n" \
    "ds_read_b64_tr_b16 v[208:209], v234 offset:49152\n" \
    "ds_read_b64_tr_b16 v[210:211], v234 offset:50176\n" \
    "ds_read_b64_tr_b16 v[212:213], v235 offset:49152\n" \
    "ds_read_b64_tr_b16 v[214:215], v235 offset:50176\n" \
    "v_mfma_f32_32x32x16_bf16 v[144:159], v[220:223], v[28:31], v[144:159]\n" \
    "buffer_load_dwordx4 v[180:183], v237, s[24:27], s63 offen offset:1024 sc1\n" \
    "s_waitcnt vmcnt(9)\n" \
    "s_waitcnt lgkmcnt(9)\n" \
    "v_mfma_f32_32x32x16_bf16 v[128:143], v[192:195], v[168:171], v[128:143]\n" \
    "ds_read_b64_tr_b16 v[216:217], v234 offset:51200\n" \
    "ds_read_b64_tr_b16 v[218:219], v234 offset:52224\n" \
    "ds_read_b64_tr_b16 v[220:221], v235 offset:51200\n" \
    "ds_read_b64_tr_b16 v[222:223], v235 offset:52224\n" \
    "v_mfma_f32_32x32x16_bf16 v[144:159], v[196:199], v[168:171], v[144:159]\n" \
    "buffer_load_dwordx4 v[56:59], v237, s[24:27], s64 offen sc1\n" \
    "s_waitcnt vmcnt(9)\n" \
    "s_waitcnt lgkmcnt(9)\n" \
    "v_mfma_f32_32x32x16_bf16 v[128:143], v[200:203], v[32:35], v[128:143]\n" \
    "ds_read_b64_tr_b16 v[192:193], v236 offset:0\n" \
    "ds_read_b64_tr_b16 v[194:195], v236 offset:1024\n" \
    "ds_read_b64_tr_b16 v[196:197], v246 offset:0\n" \
    "ds_read_b64_tr_b16 v[198:199], v246 offset:1024\n" \
    "v_mfma_f32_32x32x16_bf16 v[144:159], v[204:207], v[32:35], v[144:159]\n" \
    "buffer_load_dwordx4 v[60:63], v237, s[24:27], s64 offen offset:1024 sc1\n" \
    "s_waitcnt vmcnt(9)\n" \
    "s_waitcnt lgkmcnt(8)\n" \
    "v_mfma_f32_32x32x16_bf16 v[128:143], v[208:211], v[36:39], v[128:143]\n" \
    "ds_read_b64_tr_b16 v[200:201], v236 offset:2048\n" \
    "ds_read_b64_tr_b16 v[202:203], v236 offset:3072\n" \
    "ds_read_b64_tr_b16 v[204:205], v246 offset:2048\n" \
    "ds_read_b64_tr_b16 v[206:207], v246 offset:3072\n" \
    "v_mfma_f32_32x32x16_bf16 v[144:159], v[212:215], v[36:39], v[144:159]\n" \
    "buffer_load_dwordx4 v[184:187], v237, s[24:27], s65 offen sc1\n" \
    "s_waitcnt vmcnt(9)\n" \
    "s_waitcnt lgkmcnt(8)\n" \
    "v_mfma_f32_32x32x16_bf16 v[128:143], v[216:219], v[172:175], v[128:143]\n" \
    "ds_read_b64_tr_b16 v[208:209], v236 offset:4096\n" \
    "ds_read_b64_tr_b16 v[210:211], v236 offset:5120\n" \
    "ds_read_b64_tr_b16 v[212:213], v246 offset:4096\n" \
    "ds_read_b64_tr_b16 v[214:215], v246 offset:5120\n" \
    "v_mfma_f32_32x32x16_bf16 v[144:159], v[220:223], v[172:175], v[144:159]\n" \
    "buffer_load_dwordx4 v[64:67], v237, s[24:27], s65 offen offset:1024 sc1\n" \
    "s_waitcnt vmcnt(9)\n" \
    "s_waitcnt lgkmcnt(8)\n" \
    "v_mfma_f32_32x32x16_bf16 v[128:143], v[192:195], v[40:43], v[128:143]\n" \
    "ds_read_b64_tr_b16 v[216:217], v236 offset:6144\n" \
    "ds_read_b64_tr_b16 v[218:219], v236 offset:7168\n" \
    "ds_read_b64_tr_b16 v[220:221], v246 offset:6144\n" \
    "ds_read_b64_tr_b16 v[222:223], v246 offset:7168\n" \
    "v_mfma_f32_32x32x16_bf16 v[144:159], v[196:199], v[40:43], v[144:159]\n" \
    "buffer_load_dwordx4 v[68:71], v237, s[24:27], s66 offen sc1\n" \
    "s_waitcnt vmcnt(9)\n" \
    "s_waitcnt lgkmcnt(8)\n" \
    "v_mfma_f32_32x32x16_bf16 v[128:143], v[200:203], v[44:47], v[128:143]\n" \
    "v_mfma_f32_32x32x16_bf16 v[144:159], v[204:207], v[44:47], v[144:159]\n" \
    "buffer_load_dwordx4 v[0:3], v237, s[24:27], s66 offen offset:1024 sc1\n" \
    "s_waitcnt vmcnt(9)\n" \
    "s_waitcnt lgkmcnt(4)\n" \
    "v_mfma_f32_32x32x16_bf16 v[128:143], v[208:211], v[176:179], v[128:143]\n" \
    "v_mfma_f32_32x32x16_bf16 v[144:159], v[212:215], v[176:179], v[144:159]\n" \
    "buffer_load_dwordx4 v[72:75], v237, s[24:27], s67 offen sc1\n" \
    "s_waitcnt vmcnt(9)\n" \
    "s_waitcnt lgkmcnt(0)\n" \
    "v_mfma_f32_32x32x16_bf16 v[128:143], v[216:219], v[48:51], v[128:143]\n" \
    "ds_read_b64_tr_b16 v[192:193], v234 offset:0\n" \
    "ds_read_b64_tr_b16 v[194:195], v234 offset:1024\n" \
    "ds_read_b64_tr_b16 v[196:197], v235 offset:0\n" \
    "ds_read_b64_tr_b16 v[198:199], v235 offset:1024\n" \
    "v_mfma_f32_32x32x16_bf16 v[144:159], v[220:223], v[48:51], v[144:159]\n" \
    "buffer_load_dwordx4 v[76:79], v237, s[24:27], s67 offen offset:1024 sc1\n" \
    "s_waitcnt vmcnt(9)\n" \
    "v_mfma_f32_32x32x16_bf16 v[96:111], v[80:83], v[52:55], v[96:111]\n" \
    "ds_read_b64_tr_b16 v[200:201], v234 offset:2048\n" \
    "ds_read_b64_tr_b16 v[202:203], v234 offset:3072\n" \
    "ds_read_b64_tr_b16 v[204:205], v235 offset:2048\n" \
    "ds_read_b64_tr_b16 v[206:207], v235 offset:3072\n" \
    "v_mfma_f32_32x32x16_bf16 v[112:127], v[84:87], v[52:55], v[112:127]\n" \
    "buffer_load_dwordx4 v[4:7], v237, s[24:27], s68 offen sc1\n" \
    "s_waitcnt vmcnt(9)\n" \
    "v_mfma_f32_32x32x16_bf16 v[96:111], v[88:91], v[180:183], v[96:111]\n" \
    "ds_read_b64_tr_b16 v[208:209], v234 offset:16384\n" \
    "ds_read_b64_tr_b16 v[210:211], v234 offset:17408\n" \
    "ds_read_b64_tr_b16 v[212:213], v235 offset:16384\n" \
    "ds_read_b64_tr_b16 v[214:215], v235 offset:17408\n" \
    "v_mfma_f32_32x32x16_bf16 v[112:127], v[92:95], v[180:183], v[112:127]\n" \
    "buffer_load_dwordx4 v[188:191], v237, s[24:27], s68 offen offset:1024 sc1\n" \
    "s_waitcnt vmcnt(9)\n" \
    "s_waitcnt lgkmcnt(8)\n" \
    "v_mfma_f32_32x32x16_bf16 v[96:111], v[192:195], v[56:59], v[96:111]\n" \
    "ds_read_b64_tr_b16 v[216:217], v234 offset:18432\n" \
    "ds_read_b64_tr_b16 v[218:219], v234 offset:19456\n" \
    "ds_read_b64_tr_b16 v[220:221], v235 offset:18432\n" \
    "ds_read_b64_tr_b16 v[222:223], v235 offset:19456\n" \
    "v_mfma_f32_32x32x16_bf16 v[112:127], v[196:199], v[56:59], v[112:127]\n" \
    "buffer_load_dwordx4 v[8:11], v237, s[24:27], s69 offen sc1\n" \
    "ds_write_b128 v228, v[128:131] offset:8192\n" \
    "ds_write_b128 v229, v[132:135] offset:8192\n" \
    "s_waitcnt vmcnt(9)\n" \
    "s_waitcnt lgkmcnt(10)\n" \
    "v_mfma_f32_32x32x16_bf16 v[96:111], v[200:203], v[60:63], v[96:111]\n" \
    "ds_read_b64_tr_b16 v[192:193], v234 offset:32768\n" \
    "ds_read_b64_tr_b16 v[194:195], v234 offset:33792\n" \
    "ds_read_b64_tr_b16 v[196:197], v235 offset:32768\n" \
    "ds_read_b64_tr_b16 v[198:199], v235 offset:33792\n" \
    "v_mfma_f32_32x32x16_bf16 v[112:127], v[204:207], v[60:63], v[112:127]\n" \
    "buffer_load_dwordx4 v[12:15], v237, s[24:27], s69 offen offset:1024 sc1\n" \
    "ds_write_b128 v230, v[136:139] offset:8192\n" \
    "ds_write_b128 v231, v[140:143] offset:8192\n" \
    "s_waitcnt vmcnt(9)\n" \
    "s_waitcnt lgkmcnt(12)\n" \
    "v_mfma_f32_32x32x16_bf16 v[96:111], v[208:211], v[184:187], v[96:111]\n" \
    "ds_read_b64_tr_b16 v[200:201], v234 offset:34816\n" \
    "ds_read_b64_tr_b16 v[202:203], v234 offset:35840\n" \
    "ds_read_b64_tr_b16 v[204:205], v235 offset:34816\n" \
    "ds_read_b64_tr_b16 v[206:207], v235 offset:35840\n" \
    "v_mfma_f32_32x32x16_bf16 v[112:127], v[212:215], v[184:187], v[112:127]\n" \
    "ds_write_b128 v228, v[144:147] offset:12288\n" \
    "ds_write_b128 v229, v[148:151] offset:12288\n" \
    "s_waitcnt vmcnt(8)\n" \
    "s_waitcnt lgkmcnt(14)\n" \
    "v_mfma_f32_32x32x16_bf16 v[96:111], v[216:219], v[64:67], v[96:111]\n" \
    "ds_read_b64_tr_b16 v[208:209], v234 offset:49152\n" \
    "ds_read_b64_tr_b16 v[210:211], v234 offset:50176\n" \
    "ds_read_b64_tr_b16 v[212:213], v235 offset:49152\n" \
    "ds_read_b64_tr_b16 v[214:215], v235 offset:50176\n" \
    "v_mfma_f32_32x32x16_bf16 v[112:127], v[220:223], v[64:67], v[112:127]\n" \
    "ds_write_b128 v230, v[152:155] offset:12288\n" \
    "ds_write_b128 v231, v[156:159] offset:12288\n" \
    "v_mov_b32 v241, 1\n" \
    "ds_write_b32 v239, v241 offset:48\n" \
    "s_waitcnt vmcnt(7)\n" \
    "s_waitcnt lgkmcnt(15)\n" \
    "v_mfma_f32_32x32x16_bf16 v[96:111], v[192:195], v[68:71], v[96:111]\n" \
    "ds_read_b64_tr_b16 v[216:217], v234 offset:51200\n" \
    "ds_read_b64_tr_b16 v[218:219], v234 offset:52224\n" \
    "ds_read_b64_tr_b16 v[220:221], v235 offset:51200\n" \
    "ds_read_b64_tr_b16 v[222:223], v235 offset:52224\n" \
    "v_mfma_f32_32x32x16_bf16 v[112:127], v[196:199], v[68:71], v[112:127]\n" \
    "s_waitcnt vmcnt(6)\n" \
    "s_waitcnt lgkmcnt(13)\n" \
    "v_mfma_f32_32x32x16_bf16 v[96:111], v[200:203], v[0:3], v[96:111]\n" \
    "ds_read_b64_tr_b16 v[192:193], v236 offset:0\n" \
    "ds_read_b64_tr_b16 v[194:195], v236 offset:1024\n" \
    "ds_read_b64_tr_b16 v[196:197], v246 offset:0\n" \
    "ds_read_b64_tr_b16 v[198:199], v246 offset:1024\n" \
    "v_mfma_f32_32x32x16_bf16 v[112:127], v[204:207], v[0:3], v[112:127]\n" \
    "s_waitcnt vmcnt(5)\n" \
    "s_waitcnt lgkmcnt(11)\n" \
    "v_mfma_f32_32x32x16_bf16 v[96:111], v[208:211], v[72:75], v[96:111]\n" \
    "ds_read_b64_tr_b16 v[200:201], v236 offset:2048\n" \
    "ds_read_b64_tr_b16 v[202:203], v236 offset:3072\n" \
    "ds_read_b64_tr_b16 v[204:205], v246 offset:2048\n" \
    "ds_read_b64_tr_b16 v[206:207], v246 offset:3072\n" \
    "v_mfma_f32_32x32x16_bf16 v[112:127], v[212:215], v[72:75], v[112:127]\n" \
    "s_waitcnt vmcnt(4)\n" \
    "s_waitcnt lgkmcnt(8)\n" \
    "v_mfma_f32_32x32x16_bf16 v[96:111], v[216:219], v[76:79], v[96:111]\n" \
    "ds_read_b64_tr_b16 v[208:209], v236 offset:4096\n" \
    "ds_read_b64_tr_b16 v[210:211], v236 offset:5120\n" \
    "ds_read_b64_tr_b16 v[212:213], v246 offset:4096\n" \
    "ds_read_b64_tr_b16 v[214:215], v246 offset:5120\n" \
    "v_mfma_f32_32x32x16_bf16 v[112:127], v[220:223], v[76:79], v[112:127]\n" \
    "s_waitcnt vmcnt(3)\n" \
    "s_waitcnt lgkmcnt(8)\n" \
    "v_mfma_f32_32x32x16_bf16 v[96:111], v[192:195], v[4:7], v[96:111]\n" \
    "ds_read_b64_tr_b16 v[216:217], v236 offset:6144\n" \
    "ds_read_b64_tr_b16 v[218:219], v236 offset:7168\n" \
    "ds_read_b64_tr_b16 v[220:221], v246 offset:6144\n" \
    "ds_read_b64_tr_b16 v[222:223], v246 offset:7168\n" \
    "s_waitcnt vmcnt(2)\n" \
    "s_waitcnt lgkmcnt(8)\n" \
    "v_mfma_f32_32x32x16_bf16 v[96:111], v[200:203], v[188:191], v[96:111]\n" \
    "s_waitcnt vmcnt(1)\n" \
    "s_waitcnt lgkmcnt(4)\n" \
    "v_mfma_f32_32x32x16_bf16 v[96:111], v[208:211], v[8:11], v[96:111]\n" \
    "s_waitcnt vmcnt(0)\n" \
    "s_waitcnt lgkmcnt(0)\n" \
    "v_mfma_f32_32x32x16_bf16 v[96:111], v[216:219], v[12:15], v[96:111]\n" \
    "v_mfma_f32_32x32x16_bf16 v[112:127], v[196:199], v[4:7], v[112:127]\n" \
    "v_mfma_f32_32x32x16_bf16 v[112:127], v[204:207], v[188:191], v[112:127]\n" \
    "s_nop 9\n" \
    "ds_write_b128 v228, v[96:99] offset:0\n" \
    "ds_write_b128 v229, v[100:103] offset:0\n" \
    "v_mfma_f32_32x32x16_bf16 v[112:127], v[212:215], v[8:11], v[112:127]\n" \
    "ds_write_b128 v230, v[104:107] offset:0\n" \
    "ds_write_b128 v231, v[108:111] offset:0\n" \
    "v_mfma_f32_32x32x16_bf16 v[112:127], v[220:223], v[12:15], v[112:127]\n" \
    "s_nop 11\n" \
    "ds_write_b128 v228, v[112:115] offset:4096\n" \
    "ds_write_b128 v229, v[116:119] offset:4096\n" \
    "ds_write_b128 v230, v[120:123] offset:4096\n" \
    "ds_write_b128 v231, v[124:127] offset:4096\n" \
    "v_mov_b32 v241, 2\n" \
    "ds_write_b32 v239, v241 offset:48\n" \
    "s_waitcnt lgkmcnt(0)\n" \
    "Labort:\n" \
    "s_endpgm\n" \
    "Lhx:\n" \
    "v_lshrrev_b32 v2, 4, v255\n" \
    "v_and_b32 v3, 15, v255\n" \
    "v_and_b32 v4, 3, v255\n" \
    "v_lshlrev_b32 v4, 2, v4\n" \
    "v_add_u32 v238, 163328, v4\n" \
    "s_lshl_b32 s8, s36, 2\n" \
    "s_add_u32 s8, s8, 163328\n" \
    "v_mov_b32 v241, s8\n" \
    "v_lshlrev_b32 v4, 2, v255\n" \
    "v_add_u32 v4, 163456, v4\n" \
    "v_cmp_eq_u32 vcc, 0, v255\n" \
    "s_mov_b32 s70, 0x20000\n" \
    "s_lshl_b32 s71, s36, 12\n" \
    "v_cndmask_b32 v239, v4, v241, vcc\n" \
    "v_lshl_add_u32 v4, v2, 4, s5\n" \
    "s_barrier\n" \
    "buffer_load_dwordx4 v1, s[12:15], s4 offen lds\n" \
    "s_add_u32 m0, m0, 1024\n" \
    "s_add_u32 s4, s4, 8192\n" \
    "buffer_load_dwordx4 v1, s[12:15], s4 offen lds\n" \
    "s_add_u32 m0, m0, 1024\n" \
    "s_add_u32 s4, s4, 8192\n" \
    "buffer_load_dwordx4 v1, s[12:15], s4 offen lds\n" \
    "s_add_u32 m0, m0, 1024\n" \
    "s_add_u32 s4, s4, 8192\n" \
    "buffer_load_dwordx4 v1, s[12:15], s4 offen lds\n" \
    "s_add_u32 m0, m0, 1024\n" \
    "s_add_u32 s4, s4, 8192\n" \
    "buffer_load_dwordx4 v1, s[12:15], s4 offen lds\n" \
    "s_add_u32 m0, m0, 1024\n" \
    "s_add_u32 s4, s4, 8192\n" \
    "buffer_load_dwordx4 v1, s[12:15], s4 offen lds\n" \
    "s_add_u32 m0, m0, 1024\n" \
    "s_add_u32 s4, s4, 8192\n" \
    "buffer_load_dwordx4 v1, s[12:15], s4 offen lds\n" \
    "s_add_u32 m0, m0, 1024\n" \
    "s_add_u32 s4, s4, 8192\n" \
    "buffer_load_dwordx4 v1, s[12:15], s4 offen lds\n" \
    "s_add_u32 m0, m0, 1024\n" \
    "s_add_u32 s4, s4, 8192\n" \
    "buffer_load_dwordx4 v1, s[12:15], s4 offen lds\n" \
    "s_add_u32 m0, m0, 1024\n" \
    "s_add_u32 s4, s4, 8192\n" \
    "buffer_load_dwordx4 v1, s[12:15], s4 offen lds\n" \
    "s_add_u32 m0, m0, 1024\n" \
    "s_add_u32 s4, s4, 8192\n" \
    "buffer_load_dwordx4 v1, s[12:15], s4 offen lds\n" \
    "s_add_u32 m0, m0, 1024\n" \
    "s_add_u32 s4, s4, 8192\n" \
    "buffer_load_dwordx4 v1, s[12:15], s4 offen lds\n" \
    "global_load_dwordx4 v[12:15], v4, s[38:39]\n" \
    "global_load_dwordx4 v[16:19], v4, s[38:39] offset:64\n" \
    "v_bfe_u32 v4, v255, 1, 1\n" \
    "v_lshl_add_u32 v4, v2, 1, v4\n" \
    "v_bfe_u32 v5, v255, 3, 1\n" \
    "v_lshlrev_b32 v5, 2, v5\n" \
    "v_xor_b32 v4, v4, v5\n" \
    "v_lshrrev_b32 v5, 2, v3\n" \
    "v_lshl_add_u32 v4, v5, 3, v4\n" \
    "v_and_b32 v5, 1, v255\n" \
    "v_lshl_add_u32 v4, v4, 1, v5\n" \
    "v_lshl_add_u32 v6, v4, 3, s72\n" \
    "v_and_b32 v4, 7, v3\n" \
    "v_lshl_add_u32 v4, v3, 4, v4\n" \
    "v_lshl_add_u32 v4, v2, 10, v4\n" \
    "v_lshl_add_u32 v7, v4, 4, s71\n" \
    "v_xor_b32 v4, v2, v3\n" \
    "v_lshl_add_u32 v4, v2, 4, v4\n" \
    "v_lshl_add_u32 v8, v4, 4, s71\n" \
    "v_xor_b32 v9, 64, v8\n" \
    "v_lshlrev_b32 v4, 4, v3\n" \
    "v_lshl_add_u32 v10, v2, 12, v4\n" \
    "s_lshl_b32 s4, s76, 18\n" \
    "s_lshl_b32 s5, s75, 8\n" \
    "s_add_u32 s4, s4, s5\n" \
    "s_add_u32 s4, s4, 7864320\n" \
    "v_xor_b32 v20, 0, v7\n" \
    "v_xor_b32 v21, 16, v7\n" \
    "v_xor_b32 v22, 32, v7\n" \
    "v_xor_b32 v23, 48, v7\n" \
    "v_xor_b32 v24, 64, v7\n" \
    "v_xor_b32 v25, 80, v7\n" \
    "v_xor_b32 v26, 96, v7\n" \
    "v_xor_b32 v27, 112, v7\n" \
    "s_waitcnt vmcnt(10)\n" \
    "ds_read_b64_tr_b16 v[32:33], v6 offset:0\n" \
    "ds_read_b64_tr_b16 v[34:35], v6 offset:512\n" \
    "ds_read_b64_tr_b16 v[36:37], v6 offset:1024\n" \
    "ds_read_b64_tr_b16 v[38:39], v6 offset:1536\n" \
    "ds_read_b64_tr_b16 v[40:41], v6 offset:2048\n" \
    "ds_read_b64_tr_b16 v[42:43], v6 offset:2560\n" \
    "ds_read_b64_tr_b16 v[44:45], v6 offset:3072\n" \
    "ds_read_b64_tr_b16 v[46:47], v6 offset:3584\n" \
    "ds_read_b64_tr_b16 v[48:49], v6 offset:4096\n" \
    "ds_read_b64_tr_b16 v[50:51], v6 offset:4608\n" \
    "ds_read_b64_tr_b16 v[52:53], v6 offset:5120\n" \
    "ds_read_b64_tr_b16 v[54:55], v6 offset:5632\n" \
    "ds_read_b64_tr_b16 v[56:57], v6 offset:6144\n" \
    "ds_read_b64_tr_b16 v[58:59], v6 offset:6656\n" \
    "ds_read_b64_tr_b16 v[60:61], v6 offset:7168\n" \
    "ds_read_b64_tr_b16 v[62:63], v6 offset:7680\n" \
    "s_waitcnt lgkmcnt(14)\n" \
    "ds_write_b128 v20, v[32:35] offset:0\n" \
    "s_waitcnt lgkmcnt(13)\n" \
    "ds_write_b128 v21, v[36:39] offset:0\n" \
    "s_waitcnt lgkmcnt(12)\n" \
    "ds_write_b128 v22, v[40:43] offset:0\n" \
    "s_waitcnt lgkmcnt(11)\n" \
    "ds_write_b128 v23, v[44:47] offset:0\n" \
    "s_waitcnt lgkmcnt(10)\n" \
    "ds_write_b128 v24, v[48:51] offset:0\n" \
    "s_waitcnt lgkmcnt(9)\n" \
    "ds_write_b128 v25, v[52:55] offset:0\n" \
    "s_waitcnt lgkmcnt(8)\n" \
    "ds_write_b128 v26, v[56:59] offset:0\n" \
    "s_waitcnt lgkmcnt(7)\n" \
    "ds_write_b128 v27, v[60:63] offset:0\n" \
    "s_waitcnt vmcnt(2)\n" \
    "ds_read_b64_tr_b16 v[32:33], v6 offset:8192\n" \
    "ds_read_b64_tr_b16 v[34:35], v6 offset:8704\n" \
    "ds_read_b64_tr_b16 v[36:37], v6 offset:9216\n" \
    "ds_read_b64_tr_b16 v[38:39], v6 offset:9728\n" \
    "ds_read_b64_tr_b16 v[40:41], v6 offset:10240\n" \
    "ds_read_b64_tr_b16 v[42:43], v6 offset:10752\n" \
    "ds_read_b64_tr_b16 v[44:45], v6 offset:11264\n" \
    "ds_read_b64_tr_b16 v[46:47], v6 offset:11776\n" \
    "ds_read_b64_tr_b16 v[48:49], v6 offset:12288\n" \
    "ds_read_b64_tr_b16 v[50:51], v6 offset:12800\n" \
    "ds_read_b64_tr_b16 v[52:53], v6 offset:13312\n" \
    "ds_read_b64_tr_b16 v[54:55], v6 offset:13824\n" \
    "ds_read_b64_tr_b16 v[56:57], v6 offset:14336\n" \
    "ds_read_b64_tr_b16 v[58:59], v6 offset:14848\n" \
    "ds_read_b64_tr_b16 v[60:61], v6 offset:15360\n" \
    "ds_read_b64_tr_b16 v[62:63], v6 offset:15872\n" \
    "s_waitcnt lgkmcnt(14)\n" \
    "ds_write_b128 v20, v[32:35] offset:128\n" \
    "s_waitcnt lgkmcnt(13)\n" \
    "ds_write_b128 v21, v[36:39] offset:128\n" \
    "s_waitcnt lgkmcnt(12)\n" \
    "ds_write_b128 v22, v[40:43] offset:128\n" \
    "s_waitcnt lgkmcnt(11)\n" \
    "ds_write_b128 v23, v[44:47] offset:128\n" \
    "s_waitcnt lgkmcnt(10)\n" \
    "ds_write_b128 v24, v[48:51] offset:128\n" \
    "s_waitcnt lgkmcnt(9)\n" \
    "ds_write_b128 v25, v[52:55] offset:128\n" \
    "s_waitcnt lgkmcnt(8)\n" \
    "ds_write_b128 v26, v[56:59] offset:128\n" \
    "s_waitcnt lgkmcnt(7)\n" \
    "ds_write_b128 v27, v[60:63] offset:128\n" \
    "ds_read_b128 v[128:131], v8 offset:0\n" \
    "ds_read_b128 v[132:135], v9 offset:1024\n" \
    "ds_read_b128 v[136:139], v8 offset:2048\n" \
    "ds_read_b128 v[140:143], v9 offset:3072\n" \
    "ds_read_b128 v[144:147], v8 offset:16384\n" \
    "ds_read_b128 v[148:151], v9 offset:17408\n" \
    "ds_read_b128 v[152:155], v8 offset:18432\n" \
    "ds_read_b128 v[156:159], v9 offset:19456\n" \
    "ds_read_b128 v[160:163], v8 offset:32768\n" \
    "ds_read_b128 v[164:167], v9 offset:33792\n" \
    "ds_read_b128 v[168:171], v8 offset:34816\n" \
    "ds_read_b128 v[172:175], v9 offset:35840\n" \
    "ds_read_b128 v[176:179], v8 offset:49152\n" \
    "ds_read_b128 v[180:183], v9 offset:50176\n" \
    "ds_read_b128 v[184:187], v8 offset:51200\n" \
    "ds_read_b128 v[188:191], v9 offset:52224\n" \
    "s_waitcnt lgkmcnt(15)\n" \
    "buffer_store_dwordx4 v[128:131], v10, s[24:27], s4 offen sc1\n" \
    "s_add_u32 s4, s4, 16384\n" \
    "s_waitcnt lgkmcnt(14)\n" \
    "buffer_store_dwordx4 v[132:135], v10, s[24:27], s4 offen sc1\n" \
    "s_add_u32 s4, s4, 16384\n" \
    "s_waitcnt lgkmcnt(13)\n" \
    "buffer_store_dwordx4 v[136:139], v10, s[24:27], s4 offen sc1\n" \
    "s_add_u32 s4, s4, 16384\n" \
    "s_waitcnt lgkmcnt(12)\n" \
    "buffer_store_dwordx4 v[140:143], v10, s[24:27], s4 offen sc1\n" \
    "s_add_u32 s4, s4, 16384\n" \
    "s_waitcnt lgkmcnt(11)\n" \
    "buffer_store_dwordx4 v[144:147], v10, s[24:27], s4 offen sc1\n" \
    "s_add_u32 s4, s4, 16384\n" \
    "s_waitcnt lgkmcnt(10)\n" \
    "buffer_store_dwordx4 v[148:151], v10, s[24:27], s4 offen sc1\n" \
    "s_add_u32 s4, s4, 16384\n" \
    "s_waitcnt lgkmcnt(9)\n" \
    "buffer_store_dwordx4 v[152:155], v10, s[24:27], s4 offen sc1\n" \
    "s_add_u32 s4, s4, 16384\n" \
    "s_waitcnt lgkmcnt(8)\n" \
    "buffer_store_dwordx4 v[156:159], v10, s[24:27], s4 offen sc1\n" \
    "s_add_u32 s4, s4, 16384\n" \
    "s_waitcnt lgkmcnt(7)\n" \
    "buffer_store_dwordx4 v[160:163], v10, s[24:27], s4 offen sc1\n" \
    "s_add_u32 s4, s4, 16384\n" \
    "s_waitcnt lgkmcnt(6)\n" \
    "buffer_store_dwordx4 v[164:167], v10, s[24:27], s4 offen sc1\n" \
    "s_add_u32 s4, s4, 16384\n" \
    "s_waitcnt lgkmcnt(5)\n" \
    "buffer_store_dwordx4 v[168:171], v10, s[24:27], s4 offen sc1\n" \
    "s_add_u32 s4, s4, 16384\n" \
    "s_waitcnt lgkmcnt(4)\n" \
    "buffer_store_dwordx4 v[172:175], v10, s[24:27], s4 offen sc1\n" \
    "s_add_u32 s4, s4, 16384\n" \
    "s_waitcnt lgkmcnt(3)\n" \
    "buffer_store_dwordx4 v[176:179], v10, s[24:27], s4 offen sc1\n" \
    "s_add_u32 s4, s4, 16384\n" \
    "s_waitcnt lgkmcnt(2)\n" \
    "buffer_store_dwordx4 v[180:183], v10, s[24:27], s4 offen sc1\n" \
    "s_add_u32 s4, s4, 16384\n" \
    "s_waitcnt lgkmcnt(1)\n" \
    "buffer_store_dwordx4 v[184:187], v10, s[24:27], s4 offen sc1\n" \
    "s_add_u32 s4, s4, 16384\n" \
    "s_waitcnt lgkmcnt(0)\n" \
    "buffer_store_dwordx4 v[188:191], v10, s[24:27], s4 offen sc1\n" \
    "v_mov_b32 v244, 163328\n" \
    "ds_read_b96 v[244:246], v244 offset:64\n" \
    "s_waitcnt lgkmcnt(0)\n" \
    "v_cmp_eq_u32 vcc, 0, v246\n" \
    "s_cbranch_vccnz Ls32\n" \
    "Lb33:\n" \
    "v_and_b32 v4, 2, v3\n" \
    "v_lshlrev_b32 v4, 1, v4\n" \
    "v_xor_b32 v4, v4, v2\n" \
    "v_lshl_add_u32 v4, v3, 3, v4\n" \
    "v_lshl_add_u32 v4, v4, 4, s72\n" \
    "v_xor_b32 v5, 64, v4\n" \
    "ds_read_b128 v[32:35], v4 offset:0\n" \
    "ds_read_b128 v[36:39], v5 offset:0\n" \
    "ds_read_b128 v[40:43], v4 offset:2048\n" \
    "ds_read_b128 v[44:47], v5 offset:2048\n" \
    "ds_read_b128 v[48:51], v4 offset:4096\n" \
    "ds_read_b128 v[52:55], v5 offset:4096\n" \
    "ds_read_b128 v[56:59], v4 offset:6144\n" \
    "ds_read_b128 v[60:63], v5 offset:6144\n" \
    "ds_read_b128 v[64:67], v4 offset:8192\n" \
    "ds_read_b128 v[68:71], v5 offset:8192\n" \
    "ds_read_b128 v[72:75], v4 offset:10240\n" \
    "ds_read_b128 v[76:79], v5 offset:10240\n" \
    "ds_read_b128 v[80:83], v4 offset:12288\n" \
    "ds_read_b128 v[84:87], v5 offset:12288\n" \
    "ds_read_b128 v[88:91], v4 offset:14336\n" \
    "ds_read_b128 v[92:95], v5 offset:14336\n" \
    "s_waitcnt vmcnt(16)\n" \
    "s_waitcnt lgkmcnt(14)\n" \
    "v_mfma_f32_16x16x32_bf16 v[96:99], v[32:35], v[12:15], 0\n" \
    "s_waitcnt lgkmcnt(12)\n" \
    "v_mfma_f32_16x16x32_bf16 v[100:103], v[40:43], v[12:15], 0\n" \
    "s_waitcnt lgkmcnt(10)\n" \
    "v_mfma_f32_16x16x32_bf16 v[104:107], v[48:51], v[12:15], 0\n" \
    "s_waitcnt lgkmcnt(8)\n" \
    "v_mfma_f32_16x16x32_bf16 v[108:111], v[56:59], v[12:15], 0\n" \
    "s_waitcnt lgkmcnt(6)\n" \
    "v_mfma_f32_16x16x32_bf16 v[112:115], v[64:67], v[12:15], 0\n" \
    "s_waitcnt lgkmcnt(4)\n" \
    "v_mfma_f32_16x16x32_bf16 v[116:119], v[72:75], v[12:15], 0\n" \
    "s_waitcnt lgkmcnt(2)\n" \
    "v_mfma_f32_16x16x32_bf16 v[120:123], v[80:83], v[12:15], 0\n" \
    "s_waitcnt lgkmcnt(0)\n" \
    "v_mfma_f32_16x16x32_bf16 v[124:127], v[88:91], v[12:15], 0\n" \
    "v_mfma_f32_16x16x32_bf16 v[96:99], v[36:39], v[16:19], v[96:99]\n" \
    "v_mfma_f32_16x16x32_bf16 v[100:103], v[44:47], v[16:19], v[100:103]\n" \
    "v_mfma_f32_16x16x32_bf16 v[104:107], v[52:55], v[16:19], v[104:107]\n" \
    "v_mfma_f32_16x16x32_bf16 v[108:111], v[60:63], v[16:19], v[108:111]\n" \
    "v_mfma_f32_16x16x32_bf16 v[112:115], v[68:71], v[16:19], v[112:115]\n" \
    "v_mfma_f32_16x16x32_bf16 v[116:119], v[76:79], v[16:19], v[116:119]\n" \
    "v_mfma_f32_16x16x32_bf16 v[120:123], v[84:87], v[16:19], v[120:123]\n" \
    "v_mfma_f32_16x16x32_bf16 v[124:127], v[92:95], v[16:19], v[124:127]\n" \
    "v_mov_b32 v25, v244\n" \
    "v_mov_b32 v27, v244\n" \
    "v_mov_b32 v29, v244\n" \
    "v_mov_b32 v31, v244\n" \
    "v_lshrrev_b32 v4, 1, v2\n" \
    "v_lshrrev_b32 v5, 2, v3\n" \
    "v_lshl_add_u32 v4, v4, 1, v5\n" \
    "v_and_b32 v5, 1, v2\n" \
    "v_lshl_add_u32 v4, v4, 1, v5\n" \
    "v_and_b32 v5, 3, v3\n" \
    "v_lshl_add_u32 v4, v4, 2, v5\n" \
    "v_lshlrev_b32 v4, 4, v4\n" \
    "v_cmp_gt_u32 vcc, 8, v3\n" \
    "s_lshl3_add_u32 s4, s75, s76\n" \
    "s_lshl_b32 s4, s4, 10\n" \
    "v_bfrev_b32 v5, 1\n" \
    "v_cndmask_b32 v4, v5, v4, vcc\n" \
    "s_add_u32 s4, s4, 16515072\n" \
    "v_mov_b32 v24, v96\n" \
    "v_mov_b32 v26, v97\n" \
    "v_mov_b32 v28, v98\n" \
    "v_mov_b32 v30, v99\n" \
    "v_cmp_eq_u32 vcc, 1, v3\n" \
    "s_nop 1\n" \
    "v_cndmask_b32 v24, v24, v100, vcc\n" \
    "v_cndmask_b32 v26, v26, v101, vcc\n" \
    "v_cndmask_b32 v28, v28, v102, vcc\n" \
    "v_cndmask_b32 v30, v30, v103, vcc\n" \
    "v_cmp_eq_u32 vcc, 2, v3\n" \
    "s_nop 1\n" \
    "v_cndmask_b32 v24, v24, v104, vcc\n" \
    "v_cndmask_b32 v26, v26, v105, vcc\n" \
    "v_cndmask_b32 v28, v28, v106, vcc\n" \
    "v_cndmask_b32 v30, v30, v107, vcc\n" \
    "v_cmp_eq_u32 vcc, 3, v3\n" \
    "s_nop 1\n" \
    "v_cndmask_b32 v24, v24, v108, vcc\n" \
    "v_cndmask_b32 v26, v26, v109, vcc\n" \
    "v_cndmask_b32 v28, v28, v110, vcc\n" \
    "v_cndmask_b32 v30, v30, v111, vcc\n" \
    "v_cmp_eq_u32 vcc, 4, v3\n" \
    "s_nop 1\n" \
    "v_cndmask_b32 v24, v24, v112, vcc\n" \
    "v_cndmask_b32 v26, v26, v113, vcc\n" \
    "v_cndmask_b32 v28, v28, v114, vcc\n" \
    "v_cndmask_b32 v30, v30, v115, vcc\n" \
    "v_cmp_eq_u32 vcc, 5, v3\n" \
    "s_nop 1\n" \
    "v_cndmask_b32 v24, v24, v116, vcc\n" \
    "v_cndmask_b32 v26, v26, v117, vcc\n" \
    "v_cndmask_b32 v28, v28, v118, vcc\n" \
    "v_cndmask_b32 v30, v30, v119, vcc\n" \
    "v_cmp_eq_u32 vcc, 6, v3\n" \
    "s_nop 1\n" \
    "v_cndmask_b32 v24, v24, v120, vcc\n" \
    "v_cndmask_b32 v26, v26, v121, vcc\n" \
    "v_cndmask_b32 v28, v28, v122, vcc\n" \
    "v_cndmask_b32 v30, v30, v123, vcc\n" \
    "v_cmp_eq_u32 vcc, 7, v3\n" \
    "s_nop 1\n" \
    "v_cndmask_b32 v24, v24, v124, vcc\n" \
    "v_cndmask_b32 v26, v26, v125, vcc\n" \
    "v_cndmask_b32 v28, v28, v126, vcc\n" \
    "v_cndmask_b32 v30, v30, v127, vcc\n" \
    "buffer_store_dwordx4 v[24:27], v4, s[24:27], s4 offen sc1\n" \
    "buffer_store_dwordx4 v[28:31], v4, s[24:27], s4 offen offset:512 sc1\n" \
    "s_lshl4_add_u32 s4, s76, s75\n" \
    "s_lshl_b32 s4, s4, 7\n" \
    "s_add_u32 s4, s4, 7602176\n" \
    "v_lshlrev_b32 v5, 4, v255\n" \
    "s_waitcnt vmcnt(2)\n" \
    "s_mov_b64 exec, 255\n" \
    "buffer_store_dwordx4 v[244:247], v5, s[24:27], s4 offen sc1\n" \
    "s_mov_b64 exec, -1\n" \
    "s_lshr_b32 s6, s10, 3\n" \
    "s_mul_i32 s7, s6, 37\n" \
    "s_lshr_b32 s7, s7, 8\n" \
    "s_mul_i32 s8, s7, 7\n" \
    "s_sub_u32 s8, s6, s8\n" \
    "s_cmp_ge_u32 s8, 3\n" \
    "s_addc_u32 s5, s8, 0\n" \
    "s_cmp_eq_u32 s7, 4\n" \
    "s_cselect_b32 s5, 3, s5\n" \
    "s_cselect_b32 s7, s8, s7\n" \
    "s_cselect_b32 s8, 3, s8\n" \
    "s_and_b32 s4, s10, 7\n" \
    "s_lshl2_add_u32 s4, s4, s7\n" \
    "s_mov_b32 s6, 0\n" \
    "s_mul_i32 s7, s36, 7\n" \
    "s_sub_u32 s9, s6, 7\n" \
    "s_cselect_b32 s8, s6, s9\n" \
    "s_lshl_b32 s63, s8, 11\n" \
    "s_add_u32 s8, s8, s7\n" \
    "s_and_b32 s9, s8, 1\n" \
    "s_mul_i32 s9, s9, 0x8000\n" \
    "s_lshl_b32 s8, s8, 16\n" \
    "s_sub_u32 s56, s8, s9\n" \
    "s_add_u32 s6, s6, 1\n" \
    "s_sub_u32 s9, s6, 7\n" \
    "s_cselect_b32 s8, s6, s9\n" \
    "s_lshl_b32 s64, s8, 11\n" \
    "s_add_u32 s8, s8, s7\n" \
    "s_and_b32 s9, s8, 1\n" \
    "s_mul_i32 s9, s9, 0x8000\n" \
    "s_lshl_b32 s8, s8, 16\n" \
    "s_sub_u32 s57, s8, s9\n" \
    "s_add_u32 s6, s6, 1\n" \
    "s_sub_u32 s9, s6, 7\n" \
    "s_cselect_b32 s8, s6, s9\n" \
    "s_lshl_b32 s65, s8, 11\n" \
    "s_add_u32 s8, s8, s7\n" \
    "s_and_b32 s9, s8, 1\n" \
    "s_mul_i32 s9, s9, 0x8000\n" \
    "s_lshl_b32 s8, s8, 16\n" \
    "s_sub_u32 s58, s8, s9\n" \
    "s_add_u32 s6, s6, 1\n" \
    "s_sub_u32 s9, s6, 7\n" \
    "s_cselect_b32 s8, s6, s9\n" \
    "s_lshl_b32 s66, s8, 11\n" \
    "s_add_u32 s8, s8, s7\n" \
    "s_and_b32 s9, s8, 1\n" \
    "s_mul_i32 s9, s9, 0x8000\n" \
    "s_lshl_b32 s8, s8, 16\n" \
    "s_sub_u32 s59, s8, s9\n" \
    "s_add_u32 s6, s6, 1\n" \
    "s_sub_u32 s9, s6, 7\n" \
    "s_cselect_b32 s8, s6, s9\n" \
    "s_lshl_b32 s67, s8, 11\n" \
    "s_add_u32 s8, s8, s7\n" \
    "s_and_b32 s9, s8, 1\n" \
    "s_mul_i32 s9, s9, 0x8000\n" \
    "s_lshl_b32 s8, s8, 16\n" \
    "s_sub_u32 s60, s8, s9\n" \
    "s_add_u32 s6, s6, 1\n" \
    "s_sub_u32 s9, s6, 7\n" \
    "s_cselect_b32 s8, s6, s9\n" \
    "s_lshl_b32 s68, s8, 11\n" \
    "s_add_u32 s8, s8, s7\n" \
    "s_and_b32 s9, s8, 1\n" \
    "s_mul_i32 s9, s9, 0x8000\n" \
    "s_lshl_b32 s8, s8, 16\n" \
    "s_sub_u32 s61, s8, s9\n" \
    "s_add_u32 s6, s6, 1\n" \
    "s_sub_u32 s9, s6, 7\n" \
    "s_cselect_b32 s8, s6, s9\n" \
    "s_lshl_b32 s69, s8, 11\n" \
    "s_add_u32 s8, s8, s7\n" \
    "s_and_b32 s9, s8, 1\n" \
    "s_mul_i32 s9, s9, 0x8000\n" \
    "s_lshl_b32 s8, s8, 16\n" \
    "s_sub_u32 s62, s8, s9\n" \
    "v_lshrrev_b32 v120, 3, v255\n" \
    "v_and_b32 v121, 7, v255\n" \
    "v_lshrrev_b32 v122, 2, v255\n" \
    "v_and_b32 v122, 4, v122\n" \
    "v_xor_b32 v121, v121, v122\n" \
    "v_lshlrev_b32 v120, 10, v120\n" \
    "v_lshl_add_u32 v120, v121, 4, v120\n" \
    "s_lshl_b32 s8, s5, 7\n" \
    "v_add_u32 v248, s8, v120\n" \
    "v_add_u32 v249, 7168, v248\n" \
    "v_add_u32 v250, 14336, v248\n" \
    "v_add_u32 v251, 21504, v248\n" \
    "s_mul_i32 s8, s4, 229376\n" \
    "s_mul_i32 s9, s36, 14336\n" \
    "s_add_u32 s8, s8, s9\n" \
    "v_lshl_add_u32 v252, v255, 4, s8\n" \
    "v_add_u32 v253, 114688, v252\n" \
    "v_add_u32 v227, 57344, v252\n" \
    "v_add_u32 v237, 172032, v252\n" \
    "v_bfe_u32 v120, v255, 2, 2\n" \
    "v_lshrrev_b32 v121, 5, v255\n" \
    "v_lshl_add_u32 v121, v121, 2, v120\n" \
    "v_lshlrev_b32 v121, 7, v121\n" \
    "v_bfe_u32 v122, v255, 4, 1\n" \
    "v_bfe_u32 v123, v255, 1, 1\n" \
    "v_lshl_add_u32 v122, v122, 1, v123\n" \
    "v_and_b32 v120, 2, v120\n" \
    "v_lshlrev_b32 v120, 1, v120\n" \
    "v_xor_b32 v122, v122, v120\n" \
    "v_lshl_add_u32 v121, v122, 4, v121\n" \
    "v_and_b32 v120, 1, v255\n" \
    "v_lshl_add_u32 v121, v120, 3, v121\n" \
    "v_add_u32 v234, s71, v121\n" \
    "v_xor_b32 v235, 64, v234\n" \
    "v_add_u32 v236, s72, v121\n" \
    "s_lshl_b32 s77, s4, 5\n" \
    "s_mul_i32 s9, s36, 14\n" \
    "s_lshr_b32 s9, s9, 2\n" \
    "s_add_u32 s77, s77, s9\n" \
    "s_lshl_b32 s77, s77, 7\n" \
    "s_add_u32 s77, s77, 7340032\n" \
    "v_mov_b32 v243, -16\n" \
    "v_bfe_u32 v229, v255, 1, 1\n" \
    "v_and_b32 v230, 1, v255\n" \
    "v_lshl_or_b32 v229, v230, 1, v229\n" \
    "v_and_b32 v230, 4, v255\n" \
    "v_or_b32 v229, v229, v230\n" \
    "v_lshrrev_b32 v230, 5, v255\n" \
    "v_xor_b32 v229, v229, v230\n" \
    "v_and_b32 v230, 31, v255\n" \
    "v_lshlrev_b32 v230, 7, v230\n" \
    "v_lshl_add_u32 v229, v229, 4, v230\n" \
    "v_add_u32 v228, s72, v229\n" \
    "v_xor_b32 v229, 32, v228\n" \
    "v_xor_b32 v230, 64, v228\n" \
    "v_xor_b32 v231, 96, v228\n" \
    "v_xor_b32 v246, 64, v236\n" \
    "s_setprio 2\n" \
    "s_waitcnt lgkmcnt(0)\n" \
    "v_mov_b32 v241, 18\n" \
    "s_mov_b32 m0, s72\n" \
    "ds_write_b32 v239, v241\n" \
    "buffer_load_dwordx4 v248, s[20:23], s56 offen lds\n" \
    "buffer_load_dwordx4 v249, s[20:23], s56 offen offset:1024 lds\n" \
    "buffer_load_dwordx4 v250, s[20:23], s56 offen offset:2048 lds\n" \
    "buffer_load_dwordx4 v251, s[20:23], s56 offen offset:3072 lds\n" \
    "s_add_u32 m0, s72, 4096\n" \
    "s_nop 0\n" \
    "buffer_load_dwordx4 v248, s[20:23], s57 offen lds\n" \
    "buffer_load_dwordx4 v249, s[20:23], s57 offen offset:1024 lds\n" \
    "buffer_load_dwordx4 v250, s[20:23], s57 offen offset:2048 lds\n" \
    "buffer_load_dwordx4 v251, s[20:23], s57 offen offset:3072 lds\n" \
    "s_add_u32 m0, s72, 8192\n" \
    "s_nop 0\n" \
    "buffer_load_dwordx4 v248, s[20:23], s58 offen lds\n" \
    "buffer_load_dwordx4 v249, s[20:23], s58 offen offset:1024 lds\n" \
    "buffer_load_dwordx4 v250, s[20:23], s58 offen offset:2048 lds\n" \
    "buffer_load_dwordx4 v251, s[20:23], s58 offen offset:3072 lds\n" \
    "s_add_u32 m0, s72, 12288\n" \
    "s_nop 0\n" \
    "buffer_load_dwordx4 v248, s[20:23], s59 offen lds\n" \
    "buffer_load_dwordx4 v249, s[20:23], s59 offen offset:1024 lds\n" \
    "buffer_load_dwordx4 v250, s[20:23], s59 offen offset:2048 lds\n" \
    "buffer_load_dwordx4 v251, s[20:23], s59 offen offset:3072 lds\n" \
    "s_waitcnt vmcnt(12)\n" \
    "ds_read_b64_tr_b16 v[0:1], v236 offset:0\n" \
    "ds_read_b64_tr_b16 v[2:3], v236 offset:1024\n" \
    "ds_read_b64_tr_b16 v[4:5], v246 offset:0\n" \
    "ds_read_b64_tr_b16 v[6:7], v246 offset:1024\n" \
    "ds_read_b64_tr_b16 v[8:9], v236 offset:2048\n" \
    "ds_read_b64_tr_b16 v[10:11], v236 offset:3072\n" \
    "ds_read_b64_tr_b16 v[12:13], v246 offset:2048\n" \
    "ds_read_b64_tr_b16 v[14:15], v246 offset:3072\n" \
    "s_mov_b32 m0, s72\n" \
    "s_waitcnt lgkmcnt(0)\n" \
    "buffer_load_dwordx4 v248, s[20:23], s60 offen lds\n" \
    "buffer_load_dwordx4 v249, s[20:23], s60 offen offset:1024 lds\n" \
    "buffer_load_dwordx4 v250, s[20:23], s60 offen offset:2048 lds\n" \
    "buffer_load_dwordx4 v251, s[20:23], s60 offen offset:3072 lds\n" \
    "s_waitcnt vmcnt(12)\n" \
    "ds_read_b64_tr_b16 v[16:17], v236 offset:4096\n" \
    "ds_read_b64_tr_b16 v[18:19], v236 offset:5120\n" \
    "ds_read_b64_tr_b16 v[20:21], v246 offset:4096\n" \
    "ds_read_b64_tr_b16 v[22:23], v246 offset:5120\n" \
    "ds_read_b64_tr_b16 v[24:25], v236 offset:6144\n" \
    "ds_read_b64_tr_b16 v[26:27], v236 offset:7168\n" \
    "ds_read_b64_tr_b16 v[28:29], v246 offset:6144\n" \
    "ds_read_b64_tr_b16 v[30:31], v246 offset:7168\n" \
    "s_waitcnt lgkmcnt(0)\n" \
    "s_add_u32 m0, s72, 4096\n" \
    "s_add_u32 s56, s56, 65536\n" \
    "buffer_load_dwordx4 v248, s[20:23], s56 offen lds\n" \
    "buffer_load_dwordx4 v249, s[20:23], s56 offen offset:1024 lds\n" \
    "buffer_load_dwordx4 v250, s[20:23], s56 offen offset:2048 lds\n" \
    "buffer_load_dwordx4 v251, s[20:23], s56 offen offset:3072 lds\n" \
    "s_waitcnt vmcnt(12)\n" \
    "ds_read_b64_tr_b16 v[32:33], v236 offset:8192\n" \
    "ds_read_b64_tr_b16 v[34:35], v236 offset:9216\n" \
    "ds_read_b64_tr_b16 v[36:37], v246 offset:8192\n" \
    "ds_read_b64_tr_b16 v[38:39], v246 offset:9216\n" \
    "ds_read_b64_tr_b16 v[40:41], v236 offset:10240\n" \
    "ds_read_b64_tr_b16 v[42:43], v236 offset:11264\n" \
    "ds_read_b64_tr_b16 v[44:45], v246 offset:10240\n" \
    "ds_read_b64_tr_b16 v[46:47], v246 offset:11264\n" \
    "s_add_u32 m0, s72, 8192\n" \
    "s_waitcnt lgkmcnt(0)\n" \
    "buffer_load_dwordx4 v248, s[20:23], s61 offen lds\n" \
    "buffer_load_dwordx4 v249, s[20:23], s61 offen offset:1024 lds\n" \
    "buffer_load_dwordx4 v250, s[20:23], s61 offen offset:2048 lds\n" \
    "buffer_load_dwordx4 v251, s[20:23], s61 offen offset:3072 lds\n" \
    "s_waitcnt vmcnt(12)\n" \
    "ds_read_b64_tr_b16 v[48:49], v236 offset:12288\n" \
    "ds_read_b64_tr_b16 v[50:51], v236 offset:13312\n" \
    "ds_read_b64_tr_b16 v[52:53], v246 offset:12288\n" \
    "ds_read_b64_tr_b16 v[54:55], v246 offset:13312\n" \
    "ds_read_b64_tr_b16 v[56:57], v236 offset:14336\n" \
    "ds_read_b64_tr_b16 v[58:59], v236 offset:15360\n" \
    "ds_read_b64_tr_b16 v[60:61], v246 offset:14336\n" \
    "ds_read_b64_tr_b16 v[62:63], v246 offset:15360\n" \
    "s_add_u32 m0, s72, 12288\n" \
    "s_waitcnt lgkmcnt(0)\n" \
    "buffer_load_dwordx4 v248, s[20:23], s62 offen lds\n" \
    "buffer_load_dwordx4 v249, s[20:23], s62 offen offset:1024 lds\n" \
    "buffer_load_dwordx4 v250, s[20:23], s62 offen offset:2048 lds\n" \
    "buffer_load_dwordx4 v251, s[20:23], s62 offen offset:3072 lds\n" \
    "s_mov_b32 m0, s71\n" \
    "s_add_u32 s57, s57, 65536\n" \
    "buffer_load_dwordx4 v248, s[20:23], s57 offen lds\n" \
    "buffer_load_dwordx4 v249, s[20:23], s57 offen offset:1024 lds\n" \
    "buffer_load_dwordx4 v250, s[20:23], s57 offen offset:2048 lds\n" \
    "buffer_load_dwordx4 v251, s[20:23], s57 offen offset:3072 lds\n" \
    "s_add_u32 m0, s71, 16384\n" \
    "s_add_u32 s58, s58, 65536\n" \
    "buffer_load_dwordx4 v248, s[20:23], s58 offen lds\n" \
    "buffer_load_dwordx4 v249, s[20:23], s58 offen offset:1024 lds\n" \
    "buffer_load_dwordx4 v250, s[20:23], s58 offen offset:2048 lds\n" \
    "buffer_load_dwordx4 v251, s[20:23], s58 offen offset:3072 lds\n" \
    "s_waitcnt vmcnt(20)\n" \
    "ds_read_b64_tr_b16 v[64:65], v236 offset:0\n" \
    "ds_read_b64_tr_b16 v[66:67], v236 offset:1024\n" \
    "ds_read_b64_tr_b16 v[68:69], v246 offset:0\n" \
    "ds_read_b64_tr_b16 v[70:71], v246 offset:1024\n" \
    "ds_read_b64_tr_b16 v[72:73], v236 offset:2048\n" \
    "ds_read_b64_tr_b16 v[74:75], v236 offset:3072\n" \
    "ds_read_b64_tr_b16 v[76:77], v246 offset:2048\n" \
    "ds_read_b64_tr_b16 v[78:79], v246 offset:3072\n" \
    "v_and_b32 v226, 3, v255\n" \
    "v_bfe_u32 v224, v255, 2, 1\n" \
    "v_lshl_add_u32 v226, v224, 4, v226\n" \
    "v_lshlrev_b32 v226, 7, v226\n" \
    "s_waitcnt vmcnt(0)\n" \
    "s_sleep 46\n" \
    "buffer_load_dwordx2 v[224:225], v226, s[24:27], s77 offen sc1\n" \
    "s_waitcnt vmcnt(0)\n" \
    "s_waitcnt lgkmcnt(0)\n" \
    "s_branch Ljoin\n" \
    "Lssm:\n" \
    "s_mov_b32 s70, 0x20000\n" \
    "s_mov_b32 s66, 0x200000\n" \
    "s_mov_b32 s67, 0x20000\n" \
    "v_lshrrev_b32 v1, 4, v255\n" \
    "v_and_b32 v2, 15, v255\n" \
    "v_lshrrev_b32 v5, 3, v255\n" \
    "v_and_b32 v6, 7, v255\n" \
    "v_mov_b32 v8, 0\n" \
    "v_mov_b32 v9, 0\n" \
    "v_mov_b32 v10, 0\n" \
    "v_mov_b32 v11, 0\n" \
    "v_mov_b32 v12, 0\n" \
    "v_mov_b32 v13, 0\n" \
    "v_lshl_add_u32 v200, v255, 1, s29\n" \
    "v_lshl_add_u32 v229, v255, 2, s29\n" \
    "v_add_u32 v230, 1088, v229\n" \
    "v_add_u32 v231, 2176, v229\n" \
    "v_add_u32 v232, 3264, v229\n" \
    "v_mul_u32_u24 v3, 272, v2\n" \
    "v_lshl_add_u32 v3, v1, 4, v3\n" \
    "v_add_u32 v201, s29, v3\n" \
    "s_mul_i32 s4, s30, 3248\n" \
    "s_add_u32 s4, s4, s28\n" \
    "s_add_u32 s42, s4, 8704\n" \
    "v_and_b32 v3, 1, v1\n" \
    "v_lshl_add_u32 v3, v2, 1, v3\n" \
    "v_lshrrev_b32 v4, 1, v1\n" \
    "v_lshl_add_u32 v3, v4, 5, v3\n" \
    "v_lshl_add_u32 v202, v3, 4, s42\n" \
    "v_sub_u32 v3, 4, v4\n" \
    "v_lshlrev_b32 v3, v3, v2\n" \
    "v_lshl_add_u32 v3, v1, 8, v3\n" \
    "s_add_u32 s4, s42, 1024\n" \
    "v_add_u32 v203, s4, v3\n" \
    "v_mul_u32_u24 v4, 120, v4\n" \
    "v_add3_u32 v204, v203, v4, 8\n" \
    "v_lshlrev_b32 v3, 8, v1\n" \
    "v_lshl_add_u32 v3, v2, 2, v3\n" \
    "s_add_u32 s4, s42, 2048\n" \
    "v_add_u32 v205, s4, v3\n" \
    "v_lshlrev_b32 v211, 4, v255\n" \
    "s_lshl_b32 s4, s30, 11\n" \
    "s_add_u32 s4, s4, s28\n" \
    "s_add_u32 s4, s4, 1008\n" \
    "v_sub_u32 v208, s4, v211\n" \
    "v_add_u32 v209, 4160, v208\n" \
    "s_add_u32 s5, s30, 2\n" \
    "v_lshlrev_b32 v3, s5, v255\n" \
    "s_lshl_b32 s4, s30, 9\n" \
    "s_sub_u32 s4, s28, s4\n" \
    "s_add_u32 s4, s4, 10752\n" \
    "v_add_u32 v206, s4, v3\n" \
    "v_lshlrev_b32 v3, 1, v211\n" \
    "v_add_u32 v4, 512, v211\n" \
    "v_min_u32 v3, v3, v4\n" \
    "s_add_u32 s4, s28, 8688\n" \
    "v_add_u32 v3, s4, v3\n" \
    "v_add_u32 v4, 3248, v206\n" \
    "s_cmp_eq_u32 s30, 0\n" \
    "s_cselect_b64 vcc, -1, 0\n" \
    "v_cndmask_b32 v207, v3, v4, vcc\n" \
    "v_bfe_u32 v3, v255, 3, 1\n" \
    "v_mul_u32_u24 v3, 4160, v3\n" \
    "v_lshl_add_u32 v3, v6, 2, v3\n" \
    "s_add_u32 s4, s28, 4096\n" \
    "v_add_u32 v210, s4, v3\n" \
    "s_xor_b32 s5, s30, 1\n" \
    "s_lshl_b32 s4, s5, 11\n" \
    "s_add_u32 s4, s4, s28\n" \
    "s_add_u32 s69, s4, 11952\n" \
    "s_lshr_b32 s4, s10, 5\n" \
    "s_lshl1_add_u32 s4, s4, s5\n" \
    "s_lshl_b32 s4, s4, 10\n" \
    "s_add_u32 s11, s4, 7602176\n" \
    "v_lshlrev_b32 v212, 7, v6\n" \
    "s_lshr_b32 s4, s31, 1\n" \
    "s_lshl1_add_u32 s4, s10, s4\n" \
    "s_lshl1_add_u32 s5, s4, s5\n" \
    "s_lshl_b32 s5, s5, 11\n" \
    "s_add_u32 s33, s5, 7864320\n" \
    "s_lshl_b32 s4, s4, 8\n" \
    "s_add_u32 s34, s4, 12058624\n" \
    "v_and_b32 v3, 3, v255\n" \
    "s_mov_b32 s6, 163328\n" \
    "v_lshl_add_u32 v238, v3, 2, s6\n" \
    "s_xor_b32 s7, s31, 1\n" \
    "s_lshl2_add_u32 s7, s7, s6\n" \
    "v_mov_b32 v227, s7\n" \
    "s_lshl2_add_u32 s7, s31, s6\n" \
    "v_mov_b32 v3, s7\n" \
    "s_add_u32 s6, s6, 48\n" \
    "v_lshl_add_u32 v4, v255, 2, s6\n" \
    "v_cmp_eq_u32 vcc, 0, v255\n" \
    "s_nop 1\n" \
    "v_cndmask_b32 v226, v4, v3, vcc\n" \
    "v_mov_b32 v16, 0\n" \
    "v_mov_b32 v17, 0\n" \
    "v_mov_b32 v18, 0\n" \
    "v_mov_b32 v19, 0\n" \
    "s_waitcnt lgkmcnt(0)\n" \
    "s_lshr_b32 s4, s31, 1\n" \
    "s_lshl_b32 s4, s4, 4\n" \
    "s_lshr_b32 s38, s38, s4\n" \
    "s_lshl_b32 s38, s38, 16\n" \
    "s_lshr_b32 s39, s39, s4\n" \
    "s_lshl_b32 s39, s39, 16\n" \
    "s_cmp_eq_u32 s30, 0\n" \
    "s_cselect_b32 s39, s39, 0\n" \
    "v_mov_b32 v3, s38\n" \
    "v_mul_f32 v4, 0x3fb8aa3b, v3\n" \
    "v_rndne_f32 v4, v4\n" \
    "s_mov_b32 s4, 0x3fb8aa3b\n" \
    "v_fma_f32 v228, v3, s4, -v4\n" \
    "v_fmac_f32 v228, 0x32a5705f, v3\n" \
    "v_exp_f32 v228, v228\n" \
    "v_cvt_i32_f32 v4, v4\n" \
    "v_writelane_b32 v16, s39, 0\n" \
    "v_ldexp_f32 v228, v228, v4\n" \
    "v_cvt_pk_bf16_f32 v228, 0, v228\n" \
    "s_waitcnt vmcnt(0)\n" \
    "ds_read_u16_d16_hi v8, v200 offset:0\n" \
    "ds_read_u16_d16_hi v9, v200 offset:256\n" \
    "ds_read_u16_d16_hi v10, v200 offset:512\n" \
    "ds_read_u16_d16_hi v11, v200 offset:768\n" \
    "ds_read_u16_d16_hi v12, v200 offset:1024\n" \
    "ds_read_u16_d16_hi v13, v200 offset:1280\n" \
    "s_lshr_b32 s4, s10, 1\n" \
    "s_lshl2_add_u32 s4, s4, s31\n" \
    "s_mul_i32 s4, s4, 3584\n" \
    "s_and_b32 s5, s10, 1\n" \
    "s_mul_i32 s5, s5, 1792\n" \
    "s_add_u32 s4, s4, s5\n" \
    "v_min_u32 v15, 6, v255\n" \
    "v_lshlrev_b32 v15, 8, v15\n" \
    "buffer_load_dword v199, v15, s[16:19], s4 offen offset:128\n" \
    "s_lshl2_add_u32 s4, s10, s31\n" \
    "s_mul_i32 s4, s4, 1792\n" \
    "v_min_u32 v14, 13, v255\n" \
    "v_lshlrev_b32 v14, 7, v14\n" \
    "buffer_load_dword v198, v14, s[20:23], s4 offen\n" \
    "s_lshl2_add_u32 s4, s10, s31\n" \
    "s_cmp_eq_u32 s4, 1023\n" \
    "s_cbranch_scc0 Lnoft\n" \
    "v_and_b32 v14, 15, v255\n" \
    "v_lshlrev_b32 v14, 7, v14\n" \
    "s_mov_b32 s4, 7667712\n" \
    "buffer_load_dword v197, v14, s[24:27], s4 offen sc1\n" \
    "Lnoft:\n" \
    "s_cmp_lg_u32 s31, 0\n" \
    "s_cbranch_scc1 Lnotag\n" \
    "v_mov_b32 v3, s29\n" \
    "ds_read_b64 v[244:245], v3 offset:1536\n" \
    "s_mov_b32 s4, 0x7F4A7C15\n" \
    "s_mov_b32 s5, 0x9E3779B9\n" \
    "v_mov_b32 v246, 1\n" \
    "v_mov_b32 v3, 163328\n" \
    "s_lshl_b32 s6, s10, 3\n" \
    "s_add_u32 s6, s6, 7667712\n" \
    "s_waitcnt lgkmcnt(0)\n" \
    "v_lshl_add_u64 v[244:245], v[244:245], 0, s[4:5]\n" \
    "s_mov_b64 exec, 1\n" \
    "ds_write_b96 v3, v[244:246] offset:64\n" \
    "s_mov_b64 exec, -1\n" \
    "Lnotag:\n" \
    "s_waitcnt lgkmcnt(0)\n" \
    "Lkg:\n" \
    "v_mul_f32 v8, v228, v8\n" \
    "v_mul_f32 v9, v228, v9\n" \
    "v_mul_f32 v8, 0x3fb8aa3b, v8\n" \
    "v_mul_f32 v9, 0.15915494, v9\n" \
    "v_mul_f32 v144, 0x41000000, v8\n" \
    "v_mul_f32 v145, 0xc1000000, v9\n" \
    "v_mul_f32 v148, 0x42800000, v8\n" \
    "v_mul_f32 v149, 0xc2800000, v9\n" \
    "v_mul_f32 v156, 0x44000000, v8\n" \
    "v_mul_f32 v157, 0x44000000, v9\n" \
    "v_fract_f32 v145, v145\n" \
    "v_exp_f32 v144, v144\n" \
    "v_fract_f32 v149, v149\n" \
    "v_exp_f32 v148, v148\n" \
    "v_fract_f32 v153, v9\n" \
    "v_exp_f32 v152, v8\n" \
    "v_fract_f32 v157, v157\n" \
    "v_exp_f32 v156, v156\n" \
    "v_mul_f32 v10, v228, v10\n" \
    "v_mul_f32 v11, v228, v11\n" \
    "v_cos_f32 v98, v145\n" \
    "v_sin_f32 v99, v145\n" \
    "v_cos_f32 v112, v149\n" \
    "v_sin_f32 v113, v149\n" \
    "v_cos_f32 v128, v153\n" \
    "v_sin_f32 v129, v153\n" \
    "v_cos_f32 v138, v157\n" \
    "v_sin_f32 v139, v157\n" \
    "v_mul_f32 v32, v13, v11\n" \
    "v_mul_f32 v33, v13, v10\n" \
    "v_fma_f32 v32, v12, v10, -v32\n" \
    "v_fmac_f32 v33, v12, v11\n" \
    "v_mul_f32 v98, v144, v98\n" \
    "v_mul_f32 v99, v144, v99\n" \
    "v_mul_f32 v112, v148, v112\n" \
    "v_mul_f32 v113, v148, v113\n" \
    "v_mul_f32 v128, v152, v128\n" \
    "v_mul_f32 v129, v152, v129\n" \
    "v_mul_f32 v138, v156, v138\n" \
    "v_mul_f32 v139, v156, v139\n" \
    "v_mul_f32 v100, v99, v99\n" \
    "v_mul_f32 v101, v98, v99\n" \
    "v_fma_f32 v100, v98, v98, -v100\n" \
    "v_mul_f32 v101, 2.0, v101\n" \
    "v_mul_f32 v104, v101, v101\n" \
    "v_mul_f32 v105, v100, v101\n" \
    "v_fma_f32 v104, v100, v100, -v104\n" \
    "v_mul_f32 v105, 2.0, v105\n" \
    "v_mul_f32 v102, v101, v99\n" \
    "v_mul_f32 v103, v101, v98\n" \
    "v_fma_f32 v102, v100, v98, -v102\n" \
    "v_fmac_f32 v103, v100, v99\n" \
    "v_mov_b32 v144, 0x3f80\n" \
    "v_cvt_pk_bf16_f32 v145, v98, v99\n" \
    "ds_write2_b32 v229, v144, v145 offset0:0 offset1:68\n" \
    "v_cvt_pk_bf16_f32 v146, v100, v101\n" \
    "v_cvt_pk_bf16_f32 v147, v102, v103\n" \
    "ds_write2_b32 v229, v146, v147 offset0:136 offset1:204\n" \
    "v_mul_f32 v106, v105, v99\n" \
    "v_mul_f32 v107, v105, v98\n" \
    "v_fma_f32 v106, v104, v98, -v106\n" \
    "v_fmac_f32 v107, v104, v99\n" \
    "v_mul_f32 v108, v105, v101\n" \
    "v_mul_f32 v109, v105, v100\n" \
    "v_fma_f32 v108, v104, v100, -v108\n" \
    "v_fmac_f32 v109, v104, v101\n" \
    "v_mul_f32 v110, v105, v103\n" \
    "v_mul_f32 v111, v105, v102\n" \
    "v_fma_f32 v110, v104, v102, -v110\n" \
    "v_fmac_f32 v111, v104, v103\n" \
    "v_cvt_pk_bf16_f32 v148, v104, v105\n" \
    "v_cvt_pk_bf16_f32 v149, v106, v107\n" \
    "ds_write2_b32 v230, v148, v149 offset0:0 offset1:68\n" \
    "v_cvt_pk_bf16_f32 v150, v108, v109\n" \
    "v_cvt_pk_bf16_f32 v151, v110, v111\n" \
    "ds_write2_b32 v230, v150, v151 offset0:136 offset1:204\n" \
    "v_mul_f32 v114, v113, v99\n" \
    "v_mul_f32 v115, v113, v98\n" \
    "v_fma_f32 v114, v112, v98, -v114\n" \
    "v_fmac_f32 v115, v112, v99\n" \
    "v_cvt_pk_bf16_f32 v152, v112, v113\n" \
    "v_cvt_pk_bf16_f32 v153, v114, v115\n" \
    "ds_write2_b32 v231, v152, v153 offset0:0 offset1:68\n" \
    "v_mul_f32 v116, v113, v101\n" \
    "v_mul_f32 v117, v113, v100\n" \
    "v_fma_f32 v116, v112, v100, -v116\n" \
    "v_fmac_f32 v117, v112, v101\n" \
    "v_mul_f32 v118, v113, v103\n" \
    "v_mul_f32 v119, v113, v102\n" \
    "v_fma_f32 v118, v112, v102, -v118\n" \
    "v_fmac_f32 v119, v112, v103\n" \
    "v_cvt_pk_bf16_f32 v154, v116, v117\n" \
    "v_cvt_pk_bf16_f32 v155, v118, v119\n" \
    "ds_write2_b32 v231, v154, v155 offset0:136 offset1:204\n" \
    "v_mul_f32 v120, v113, v105\n" \
    "v_mul_f32 v121, v113, v104\n" \
    "v_fma_f32 v120, v112, v104, -v120\n" \
    "v_fmac_f32 v121, v112, v105\n" \
    "v_mul_f32 v122, v113, v107\n" \
    "v_mul_f32 v123, v113, v106\n" \
    "v_fma_f32 v122, v112, v106, -v122\n" \
    "v_fmac_f32 v123, v112, v107\n" \
    "v_cvt_pk_bf16_f32 v156, v120, v121\n" \
    "v_cvt_pk_bf16_f32 v157, v122, v123\n" \
    "ds_write2_b32 v232, v156, v157 offset0:0 offset1:68\n" \
    "v_mul_f32 v124, v113, v109\n" \
    "v_mul_f32 v125, v113, v108\n" \
    "v_fma_f32 v124, v112, v108, -v124\n" \
    "v_fmac_f32 v125, v112, v109\n" \
    "v_mul_f32 v126, v113, v111\n" \
    "v_mul_f32 v127, v113, v110\n" \
    "v_fma_f32 v126, v112, v110, -v126\n" \
    "v_fmac_f32 v127, v112, v111\n" \
    "v_cvt_pk_bf16_f32 v158, v124, v125\n" \
    "v_cvt_pk_bf16_f32 v159, v126, v127\n" \
    "ds_write2_b32 v232, v158, v159 offset0:136 offset1:204\n" \
    "ds_read_b128 v[160:163], v201 offset:0\n" \
    "ds_read_b128 v[164:167], v201 offset:64\n" \
    "ds_read_b128 v[168:171], v201 offset:128\n" \
    "ds_read_b128 v[172:175], v201 offset:192\n" \
    "v_mul_f32 v130, v129, v129\n" \
    "v_mul_f32 v131, v128, v129\n" \
    "v_fma_f32 v130, v128, v128, -v130\n" \
    "v_mul_f32 v131, 2.0, v131\n" \
    "v_mul_f32 v132, v131, v131\n" \
    "v_mul_f32 v133, v130, v131\n" \
    "v_fma_f32 v132, v130, v130, -v132\n" \
    "v_mul_f32 v133, 2.0, v133\n" \
    "v_mul_f32 v134, v113, v113\n" \
    "v_mul_f32 v135, v112, v113\n" \
    "v_fma_f32 v134, v112, v112, -v134\n" \
    "v_mul_f32 v135, -2.0, v135\n" \
    "v_mul_f32 v34, v33, v129\n" \
    "v_mul_f32 v35, v33, v128\n" \
    "v_fma_f32 v34, v32, v128, -v34\n" \
    "v_fmac_f32 v35, v32, v129\n" \
    "v_mul_f32 v36, v33, v131\n" \
    "v_mul_f32 v37, v33, v130\n" \
    "v_fma_f32 v36, v32, v130, -v36\n" \
    "v_fmac_f32 v37, v32, v131\n" \
    "v_mul_f32 v38, v37, v129\n" \
    "v_mul_f32 v39, v37, v128\n" \
    "v_fma_f32 v38, v36, v128, -v38\n" \
    "v_fmac_f32 v39, v36, v129\n" \
    "v_cvt_pk_bf16_f32 v144, v32, v33\n" \
    "v_cvt_pk_bf16_f32 v145, v34, v35\n" \
    "ds_write2_b32 v229, v144, v145 offset0:0 offset1:68\n" \
    "v_cvt_pk_bf16_f32 v146, v36, v37\n" \
    "v_cvt_pk_bf16_f32 v147, v38, v39\n" \
    "ds_write2_b32 v229, v146, v147 offset0:136 offset1:204\n" \
    "v_mul_f32 v40, v33, v133\n" \
    "v_mul_f32 v41, v33, v132\n" \
    "v_fma_f32 v40, v32, v132, -v40\n" \
    "v_fmac_f32 v41, v32, v133\n" \
    "v_mul_f32 v42, v35, v133\n" \
    "v_mul_f32 v43, v35, v132\n" \
    "v_fma_f32 v42, v34, v132, -v42\n" \
    "v_fmac_f32 v43, v34, v133\n" \
    "v_cvt_pk_bf16_f32 v148, v40, v41\n" \
    "v_cvt_pk_bf16_f32 v149, v42, v43\n" \
    "ds_write2_b32 v230, v148, v149 offset0:0 offset1:68\n" \
    "v_mul_f32 v44, v37, v133\n" \
    "v_mul_f32 v45, v37, v132\n" \
    "v_fma_f32 v44, v36, v132, -v44\n" \
    "v_fmac_f32 v45, v36, v133\n" \
    "v_mul_f32 v46, v39, v133\n" \
    "v_mul_f32 v47, v39, v132\n" \
    "v_fma_f32 v46, v38, v132, -v46\n" \
    "v_fmac_f32 v47, v38, v133\n" \
    "v_cvt_pk_bf16_f32 v150, v44, v45\n" \
    "v_cvt_pk_bf16_f32 v151, v46, v47\n" \
    "ds_write2_b32 v230, v150, v151 offset0:136 offset1:204\n" \
    "v_mul_f32 v48, v33, v135\n" \
    "v_mul_f32 v49, v33, v134\n" \
    "v_fma_f32 v48, v32, v134, -v48\n" \
    "v_fmac_f32 v49, v32, v135\n" \
    "v_mul_f32 v50, v35, v135\n" \
    "v_mul_f32 v51, v35, v134\n" \
    "v_fma_f32 v50, v34, v134, -v50\n" \
    "v_fmac_f32 v51, v34, v135\n" \
    "v_cvt_pk_bf16_f32 v152, v48, v49\n" \
    "v_cvt_pk_bf16_f32 v153, v50, v51\n" \
    "ds_write2_b32 v231, v152, v153 offset0:0 offset1:68\n" \
    "v_mul_f32 v52, v37, v135\n" \
    "v_mul_f32 v53, v37, v134\n" \
    "v_fma_f32 v52, v36, v134, -v52\n" \
    "v_fmac_f32 v53, v36, v135\n" \
    "v_mul_f32 v54, v39, v135\n" \
    "v_mul_f32 v55, v39, v134\n" \
    "v_fma_f32 v54, v38, v134, -v54\n" \
    "v_fmac_f32 v55, v38, v135\n" \
    "v_cvt_pk_bf16_f32 v154, v52, v53\n" \
    "v_cvt_pk_bf16_f32 v155, v54, v55\n" \
    "ds_write2_b32 v231, v154, v155 offset0:136 offset1:204\n" \
    "v_mul_f32 v56, v41, v135\n" \
    "v_mul_f32 v57, v41, v134\n" \
    "v_fma_f32 v56, v40, v134, -v56\n" \
    "v_fmac_f32 v57, v40, v135\n" \
    "v_mul_f32 v58, v43, v135\n" \
    "v_mul_f32 v59, v43, v134\n" \
    "v_fma_f32 v58, v42, v134, -v58\n" \
    "v_fmac_f32 v59, v42, v135\n" \
    "v_cvt_pk_bf16_f32 v156, v56, v57\n" \
    "v_cvt_pk_bf16_f32 v157, v58, v59\n" \
    "ds_write2_b32 v232, v156, v157 offset0:0 offset1:68\n" \
    "v_mul_f32 v60, v45, v135\n" \
    "v_mul_f32 v61, v45, v134\n" \
    "v_fma_f32 v60, v44, v134, -v60\n" \
    "v_fmac_f32 v61, v44, v135\n" \
    "v_mul_f32 v62, v47, v135\n" \
    "v_mul_f32 v63, v47, v134\n" \
    "v_fma_f32 v62, v46, v134, -v62\n" \
    "v_fmac_f32 v63, v46, v135\n" \
    "v_cvt_pk_bf16_f32 v158, v60, v61\n" \
    "v_cvt_pk_bf16_f32 v159, v62, v63\n" \
    "ds_write2_b32 v232, v158, v159 offset0:136 offset1:204\n" \
    "ds_read_b128 v[176:179], v201 offset:0\n" \
    "ds_read_b128 v[180:183], v201 offset:64\n" \
    "ds_read_b128 v[184:187], v201 offset:128\n" \
    "ds_read_b128 v[188:191], v201 offset:192\n" \
    "v_mul_f32 v136, v135, v135\n" \
    "v_mul_f32 v137, v134, v135\n" \
    "v_fma_f32 v136, v134, v134, -v136\n" \
    "v_mul_f32 v137, 2.0, v137\n" \
    "v_mul_f32 v140, v139, v139\n" \
    "v_mul_f32 v141, v138, v139\n" \
    "v_fma_f32 v140, v138, v138, -v140\n" \
    "v_mul_f32 v141, 2.0, v141\n" \
    "v_mul_f32 v64, v33, v137\n" \
    "v_mul_f32 v65, v33, v136\n" \
    "v_fma_f32 v64, v32, v136, -v64\n" \
    "v_fmac_f32 v65, v32, v137\n" \
    "v_mul_f32 v66, v37, v137\n" \
    "v_mul_f32 v67, v37, v136\n" \
    "v_fma_f32 v66, v36, v136, -v66\n" \
    "v_fmac_f32 v67, v36, v137\n" \
    "s_waitcnt lgkmcnt(0)\n" \
    "v_mfma_f32_16x16x32_bf16 v[16:19], v[176:179], v[160:163], v[16:19]\n" \
    "v_mfma_f32_16x16x32_bf16 v[16:19], v[180:183], v[164:167], v[16:19]\n" \
    "v_mfma_f32_16x16x32_bf16 v[16:19], v[184:187], v[168:171], v[16:19]\n" \
    "v_mfma_f32_16x16x32_bf16 v[16:19], v[188:191], v[172:175], v[16:19]\n" \
    "v_cvt_pk_bf16_f32 v144, v64, v65\n" \
    "v_cvt_pk_bf16_f32 v145, v66, v67\n" \
    "ds_write2_b32 v229, v144, v145 offset0:0 offset1:68\n" \
    "v_mul_f32 v68, v41, v137\n" \
    "v_mul_f32 v69, v41, v136\n" \
    "v_fma_f32 v68, v40, v136, -v68\n" \
    "v_fmac_f32 v69, v40, v137\n" \
    "v_mul_f32 v70, v45, v137\n" \
    "v_mul_f32 v71, v45, v136\n" \
    "v_fma_f32 v70, v44, v136, -v70\n" \
    "v_fmac_f32 v71, v44, v137\n" \
    "v_cvt_pk_bf16_f32 v146, v68, v69\n" \
    "v_cvt_pk_bf16_f32 v147, v70, v71\n" \
    "ds_write2_b32 v229, v146, v147 offset0:136 offset1:204\n" \
    "v_mul_f32 v72, v49, v137\n" \
    "v_mul_f32 v73, v49, v136\n" \
    "v_fma_f32 v72, v48, v136, -v72\n" \
    "v_fmac_f32 v73, v48, v137\n" \
    "v_mul_f32 v74, v53, v137\n" \
    "v_mul_f32 v75, v53, v136\n" \
    "v_fma_f32 v74, v52, v136, -v74\n" \
    "v_fmac_f32 v75, v52, v137\n" \
    "v_cvt_pk_bf16_f32 v148, v72, v73\n" \
    "v_cvt_pk_bf16_f32 v149, v74, v75\n" \
    "ds_write2_b32 v230, v148, v149 offset0:0 offset1:68\n" \
    "v_mul_f32 v76, v57, v137\n" \
    "v_mul_f32 v77, v57, v136\n" \
    "v_fma_f32 v76, v56, v136, -v76\n" \
    "v_fmac_f32 v77, v56, v137\n" \
    "v_mul_f32 v78, v61, v137\n" \
    "v_mul_f32 v79, v61, v136\n" \
    "v_fma_f32 v78, v60, v136, -v78\n" \
    "v_fmac_f32 v79, v60, v137\n" \
    "v_cvt_pk_bf16_f32 v150, v76, v77\n" \
    "v_cvt_pk_bf16_f32 v151, v78, v79\n" \
    "ds_write2_b32 v230, v150, v151 offset0:136 offset1:204\n" \
    "v_mul_f32 v80, v33, v139\n" \
    "v_mul_f32 v81, v33, v138\n" \
    "v_fma_f32 v80, v32, v138, -v80\n" \
    "v_fmac_f32 v81, v32, v139\n" \
    "v_mul_f32 v82, v41, v139\n" \
    "v_mul_f32 v83, v41, v138\n" \
    "v_fma_f32 v82, v40, v138, -v82\n" \
    "v_fmac_f32 v83, v40, v139\n" \
    "v_cvt_pk_bf16_f32 v152, v80, v81\n" \
    "v_cvt_pk_bf16_f32 v153, v82, v83\n" \
    "ds_write2_b32 v231, v152, v153 offset0:0 offset1:68\n" \
    "v_mul_f32 v84, v49, v139\n" \
    "v_mul_f32 v85, v49, v138\n" \
    "v_fma_f32 v84, v48, v138, -v84\n" \
    "v_fmac_f32 v85, v48, v139\n" \
    "v_mul_f32 v86, v57, v139\n" \
    "v_mul_f32 v87, v57, v138\n" \
    "v_fma_f32 v86, v56, v138, -v86\n" \
    "v_fmac_f32 v87, v56, v139\n" \
    "v_cvt_pk_bf16_f32 v154, v84, v85\n" \
    "v_cvt_pk_bf16_f32 v155, v86, v87\n" \
    "ds_write2_b32 v231, v154, v155 offset0:136 offset1:204\n" \
    "v_mul_f32 v88, v65, v139\n" \
    "v_mul_f32 v89, v65, v138\n" \
    "v_fma_f32 v88, v64, v138, -v88\n" \
    "v_fmac_f32 v89, v64, v139\n" \
    "v_mul_f32 v90, v69, v139\n" \
    "v_mul_f32 v91, v69, v138\n" \
    "v_fma_f32 v90, v68, v138, -v90\n" \
    "v_fmac_f32 v91, v68, v139\n" \
    "v_cvt_pk_bf16_f32 v156, v88, v89\n" \
    "v_cvt_pk_bf16_f32 v157, v90, v91\n" \
    "ds_write2_b32 v232, v156, v157 offset0:0 offset1:68\n" \
    "v_mul_f32 v92, v73, v139\n" \
    "v_mul_f32 v93, v73, v138\n" \
    "v_fma_f32 v92, v72, v138, -v92\n" \
    "v_fmac_f32 v93, v72, v139\n" \
    "v_mul_f32 v94, v77, v139\n" \
    "v_mul_f32 v95, v77, v138\n" \
    "v_fma_f32 v94, v76, v138, -v94\n" \
    "v_fmac_f32 v95, v76, v139\n" \
    "v_cvt_pk_bf16_f32 v158, v92, v93\n" \
    "v_cvt_pk_bf16_f32 v159, v94, v95\n" \
    "ds_write2_b32 v232, v158, v159 offset0:136 offset1:204\n" \
    "ds_read_b128 v[176:179], v201 offset:0\n" \
    "ds_read_b128 v[180:183], v201 offset:64\n" \
    "ds_read_b128 v[184:187], v201 offset:128\n" \
    "ds_read_b128 v[188:191], v201 offset:192\n" \
    "v_mul_f32 v96, v33, v141\n" \
    "v_mul_f32 v97, v33, v140\n" \
    "v_fma_f32 v96, v32, v140, -v96\n" \
    "v_fmac_f32 v97, v32, v141\n" \
    "v_mul_f32 v98, v49, v141\n" \
    "v_mul_f32 v99, v49, v140\n" \
    "v_fma_f32 v98, v48, v140, -v98\n" \
    "v_fmac_f32 v99, v48, v141\n" \
    "v_cvt_pk_bf16_f32 v144, v96, v97\n" \
    "v_cvt_pk_bf16_f32 v145, v98, v99\n" \
    "ds_write2_b32 v229, v144, v145 offset0:0 offset1:68\n" \
    "v_mul_f32 v100, v65, v141\n" \
    "v_mul_f32 v101, v65, v140\n" \
    "v_fma_f32 v100, v64, v140, -v100\n" \
    "v_fmac_f32 v101, v64, v141\n" \
    "s_waitcnt lgkmcnt(1)\n" \
    "v_mfma_f32_16x16x32_bf16 v[20:23], v[176:179], v[160:163], 0\n" \
    "v_mfma_f32_16x16x32_bf16 v[20:23], v[180:183], v[164:167], v[20:23]\n" \
    "v_mfma_f32_16x16x32_bf16 v[20:23], v[184:187], v[168:171], v[20:23]\n" \
    "v_mfma_f32_16x16x32_bf16 v[20:23], v[188:191], v[172:175], v[20:23]\n" \
    "v_mul_f32 v102, v73, v141\n" \
    "v_mul_f32 v103, v73, v140\n" \
    "v_fma_f32 v102, v72, v140, -v102\n" \
    "v_fmac_f32 v103, v72, v141\n" \
    "v_cvt_pk_bf16_f32 v146, v100, v101\n" \
    "v_cvt_pk_bf16_f32 v147, v102, v103\n" \
    "ds_write2_b32 v229, v146, v147 offset0:136 offset1:204\n" \
    "v_mul_f32 v104, v81, v141\n" \
    "v_mul_f32 v105, v81, v140\n" \
    "v_fma_f32 v104, v80, v140, -v104\n" \
    "v_fmac_f32 v105, v80, v141\n" \
    "v_mul_f32 v106, v85, v141\n" \
    "v_mul_f32 v107, v85, v140\n" \
    "v_fma_f32 v106, v84, v140, -v106\n" \
    "v_fmac_f32 v107, v84, v141\n" \
    "v_cvt_pk_bf16_f32 v148, v104, v105\n" \
    "v_cvt_pk_bf16_f32 v149, v106, v107\n" \
    "ds_write2_b32 v230, v148, v149 offset0:0 offset1:68\n" \
    "v_mul_f32 v108, v89, v141\n" \
    "v_mul_f32 v109, v89, v140\n" \
    "v_fma_f32 v108, v88, v140, -v108\n" \
    "v_fmac_f32 v109, v88, v141\n" \
    "v_mul_f32 v110, v93, v141\n" \
    "v_mul_f32 v111, v93, v140\n" \
    "v_fma_f32 v110, v92, v140, -v110\n" \
    "v_fmac_f32 v111, v92, v141\n" \
    "v_cvt_pk_bf16_f32 v150, v108, v109\n" \
    "v_cvt_pk_bf16_f32 v151, v110, v111\n" \
    "ds_write2_b32 v230, v150, v151 offset0:136 offset1:204\n" \
    "v_mul_f32 v112, v97, v141\n" \
    "v_mul_f32 v113, v97, v140\n" \
    "v_fma_f32 v112, v96, v140, -v112\n" \
    "v_fmac_f32 v113, v96, v141\n" \
    "v_cvt_pk_bf16_f32 v152, v112, v113\n" \
    "ds_write_b32 v231, v152 offset:0\n" \
    "ds_read_b128 v[176:179], v201 offset:0\n" \
    "ds_read_b128 v[180:183], v201 offset:64\n" \
    "ds_read_b128 v[184:187], v201 offset:128\n" \
    "ds_read_b128 v[188:191], v201 offset:192\n" \
    "ds_write_b128 v202, v[16:19]\n" \
    "ds_write_b64 v203, v[20:21]\n" \
    "ds_write_b64 v204, v[22:23]\n" \
    "s_waitcnt lgkmcnt(3)\n" \
    "v_mfma_f32_16x16x32_bf16 v[24:27], v[176:179], v[160:163], 0\n" \
    "v_mfma_f32_16x16x32_bf16 v[24:27], v[180:183], v[164:167], v[24:27]\n" \
    "v_mfma_f32_16x16x32_bf16 v[24:27], v[184:187], v[168:171], v[24:27]\n" \
    "v_mfma_f32_16x16x32_bf16 v[24:27], v[188:191], v[172:175], v[24:27]\n" \
    "s_mov_b32 exec_hi, 1\n" \
    "s_nop 6\n" \
    "ds_write2_b32 v205, v24, v25 offset0:0 offset1:16\n" \
    "ds_write2_b32 v205, v26, v27 offset0:32 offset1:48\n" \
    "s_mov_b32 exec_hi, -1\n" \
    "v_mov_b32 v241, 1\n" \
    "ds_write_b32 v226, v241 offset:80\n" \
    "v_mov_b32 v244, 163328\n" \
    "ds_read_b96 v[244:246], v244 offset:64\n" \
    "s_waitcnt lgkmcnt(0)\n" \
    "v_cmp_eq_u32 vcc, 0, v246\n" \
    "s_cbranch_vccnz Ls34\n" \
    "Lb35:\n" \
    "v_readlane_b32 s4, v24, 0\n" \
    "ds_read_b32 v130, v238 offset:0\n" \
    "Lp36:\n" \
    "ds_read_b32 v240, v227 offset:80\n" \
    "s_sub_u32 s70, s70, 1\n" \
    "s_cbranch_scc1 Labort\n" \
    "s_waitcnt lgkmcnt(0)\n" \
    "v_cmp_gt_u32 vcc, 1, v240\n" \
    "s_cbranch_vccnz Lp36\n" \
    "v_cmp_gt_u32 vcc, 9, v130\n" \
    "s_cmp_ge_u32 s10, 224\n" \
    "s_cselect_b64 s[14:15], -1, vcc\n" \
    "s_cmp_eq_u64 s[14:15], 0\n" \
    "s_cbranch_scc0 Lfu1\n" \
    "buffer_load_dwordx2 v[128:129], v212, s[24:27], s11 offen sc1\n" \
    "Lfu1:\n" \
    "s_cmp_eq_u32 s30, 0\n" \
    "s_cbranch_scc0 Lexp1\n" \
    "ds_read2_b32 v[32:33], v206 offset0:0 offset1:1\n" \
    "ds_read2_b32 v[34:35], v207 offset0:0 offset1:1\n" \
    "ds_read2_b32 v[36:37], v206 offset0:64 offset1:65\n" \
    "ds_read2_b32 v[38:39], v207 offset0:64 offset1:65\n" \
    "v_mov_b32 v3, 0\n" \
    "s_mov_b32 vcc_lo, 0x55555555\n" \
    "s_mov_b32 vcc_hi, 0x55555555\n" \
    "ds_write_b32 v210, v3\n" \
    "s_waitcnt lgkmcnt(3)\n" \
    "v_add_f32 v32, v32, v34\n" \
    "v_add_f32 v33, v33, v35\n" \
    "v_cndmask_b32 v34, v33, v32, vcc\n" \
    "v_cvt_pk_bf16_f32 v48, v33, v33\n" \
    "v_cvt_pk_bf16_f32 v49, v33, v34\n" \
    "v_cvt_pk_bf16_f32 v50, v33, v33\n" \
    "v_cvt_pk_bf16_f32 v51, v34, v32\n" \
    "v_cvt_pk_bf16_f32 v52, v32, v32\n" \
    "v_cvt_pk_bf16_f32 v53, v32, v32\n" \
    "ds_write2_b64 v208, v[48:49], v[52:53] offset0:128 offset1:129\n" \
    "ds_write2_b64 v209, v[50:51], v[52:53] offset0:128 offset1:129\n" \
    "s_waitcnt lgkmcnt(3)\n" \
    "v_add_f32 v36, v36, v38\n" \
    "v_add_f32 v37, v37, v39\n" \
    "v_cndmask_b32 v38, v37, v36, vcc\n" \
    "v_cvt_pk_bf16_f32 v56, v37, v37\n" \
    "v_cvt_pk_bf16_f32 v57, v37, v38\n" \
    "v_cvt_pk_bf16_f32 v58, v37, v37\n" \
    "v_cvt_pk_bf16_f32 v59, v38, v36\n" \
    "v_cvt_pk_bf16_f32 v60, v36, v36\n" \
    "v_cvt_pk_bf16_f32 v61, v36, v36\n" \
    "ds_write2_b64 v208, v[56:57], v[60:61] offset0:0 offset1:1\n" \
    "ds_write2_b64 v209, v[58:59], v[60:61] offset0:0 offset1:1\n" \
    "s_branch Lexp2\n" \
    "Lexp1:\n" \
    "ds_read_b64 v[32:33], v206 offset:0\n" \
    "ds_read_b32 v34, v206 offset:8\n" \
    "ds_read_b64 v[36:37], v206 offset:3248\n" \
    "ds_read_b32 v38, v206 offset:3256\n" \
    "ds_read_b32 v51, v207 offset:12\n" \
    "ds_read_b128 v[52:55], v207 offset:16\n" \
    "ds_read_b128 v[56:59], v207 offset:32\n" \
    "ds_read_b32 v63, v207 offset:3260\n" \
    "ds_read_b128 v[64:67], v207 offset:3264\n" \
    "ds_read_b128 v[68:71], v207 offset:3280\n" \
    "s_waitcnt lgkmcnt(6)\n" \
    "v_writelane_b32 v38, s4, 63\n" \
    "v_add_f32 v32, v32, v36\n" \
    "v_add_f32 v33, v33, v37\n" \
    "v_add_f32 v34, v34, v38\n" \
    "v_cvt_pk_bf16_f32 v80, v34, v34\n" \
    "v_cvt_pk_bf16_f32 v81, v33, v33\n" \
    "v_cvt_pk_bf16_f32 v82, v33, v32\n" \
    "v_cvt_pk_bf16_f32 v83, v32, v32\n" \
    "v_cvt_pk_bf16_f32 v84, v34, v33\n" \
    "v_cvt_pk_bf16_f32 v85, v33, v33\n" \
    "v_cvt_pk_bf16_f32 v86, v32, v32\n" \
    "v_cvt_pk_bf16_f32 v87, v32, v32\n" \
    "ds_write_b128 v208, v[80:83]\n" \
    "ds_write_b128 v209, v[84:87]\n" \
    "s_mov_b32 vcc_lo, 0\n" \
    "s_mov_b32 vcc_hi, -1\n" \
    "s_mov_b32 s4, 0\n" \
    "s_mov_b32 s5, -2\n" \
    "s_waitcnt lgkmcnt(2)\n" \
    "v_add_f32 v51, v51, v63\n" \
    "v_add_f32 v52, v52, v64\n" \
    "v_add_f32 v53, v53, v65\n" \
    "v_add_f32 v54, v54, v66\n" \
    "v_add_f32 v55, v55, v67\n" \
    "v_add_f32 v56, v56, v68\n" \
    "v_add_f32 v57, v57, v69\n" \
    "v_add_f32 v58, v58, v70\n" \
    "v_add_f32 v59, v59, v71\n" \
    "v_cndmask_b32 v96, v59, v56, vcc\n" \
    "v_cndmask_b32 v97, v58, v55, vcc\n" \
    "v_cndmask_b32 v98, v57, v54, vcc\n" \
    "v_cndmask_b32 v99, v56, v54, vcc\n" \
    "v_cndmask_b32 v100, v55, v54, vcc\n" \
    "v_cndmask_b32 v101, v54, v53, vcc\n" \
    "v_cndmask_b32 v102, v53, v52, vcc\n" \
    "v_cndmask_b32_e64 v104, v51, v52, s[4:5]\n" \
    "v_cvt_pk_bf16_f32 v88, v96, v97\n" \
    "v_cvt_pk_bf16_f32 v89, v98, v99\n" \
    "v_cvt_pk_bf16_f32 v90, v100, v101\n" \
    "v_cvt_pk_bf16_f32 v91, v102, v52\n" \
    "ds_write_b128 v208, v[88:91] offset:1024\n" \
    "v_cvt_pk_bf16_f32 v92, v97, v98\n" \
    "v_cvt_pk_bf16_f32 v93, v99, v100\n" \
    "v_cvt_pk_bf16_f32 v94, v101, v102\n" \
    "v_cvt_pk_bf16_f32 v95, v52, v104\n" \
    "ds_write_b128 v209, v[92:95] offset:1024\n" \
    "Lexp2:\n" \
    "s_cmp_eq_u64 s[14:15], 0\n" \
    "s_cbranch_scc1 Lfu2\n" \
    "Lp37:\n" \
    "ds_read_b32 v240, v238 offset:0\n" \
    "s_sub_u32 s70, s70, 1\n" \
    "s_cbranch_scc1 Labort\n" \
    "s_waitcnt lgkmcnt(0)\n" \
    "v_cmp_gt_u32 vcc, 9, v240\n" \
    "s_cbranch_vccz Ld38\n" \
    "s_sleep 1\n" \
    "s_branch Lp37\n" \
    "Ld38:\n" \
    "buffer_load_dwordx2 v[128:129], v212, s[24:27], s11 offen sc1\n" \
    "Lfu2:\n" \
    "s_waitcnt vmcnt(0)\n" \
    "Lk40:\n" \
    "v_cmp_ne_u64 vcc, v[128:129], v[244:245]\n" \
    "s_mov_b32 m0, s69\n" \
    "s_cbranch_vccnz Ls39\n" \
    "buffer_load_dwordx4 v211, s[24:27], s33 offen offset:0 sc1 lds\n" \
    "buffer_load_dwordx4 v211, s[24:27], s33 offen offset:1024 sc1 lds\n" \
    "v_and_b32 v3, 1, v2\n" \
    "v_mul_u32_u24 v3, 4158, v3\n" \
    "v_lshl_add_u32 v3, v2, 1, v3\n" \
    "v_lshlrev_b32 v4, 4, v1\n" \
    "v_sub_u32 v3, v4, v3\n" \
    "s_add_u32 s4, s28, 7772\n" \
    "v_add_u32 v233, s4, v3\n" \
    "v_subrev_u32 v234, 512, v233\n" \
    "v_subrev_u32 v235, 1024, v233\n" \
    "v_subrev_u32 v236, 1536, v233\n" \
    "v_subrev_u32 v237, 2048, v233\n" \
    "v_subrev_u32 v248, 2560, v233\n" \
    "v_subrev_u32 v249, 3072, v233\n" \
    "v_subrev_u32 v250, 3584, v233\n" \
    "v_lshl_add_u32 v3, v2, 5, v4\n" \
    "s_add_u32 s4, s28, 11472\n" \
    "v_add_u32 v214, s4, v3\n" \
    "v_and_b32 v3, 3, v255\n" \
    "v_lshl_add_u32 v3, v3, 2, v1\n" \
    "v_bfe_u32 v4, v255, 2, 2\n" \
    "v_lshl_add_u32 v3, v3, 2, v4\n" \
    "v_lshlrev_b32 v215, 2, v3\n" \
    "v_lshlrev_b32 v3, 4, v2\n" \
    "s_mov_b32 s4, 131712\n" \
    "v_mad_u32_u24 v216, v1, s4, v3\n" \
    "s_lshr_b32 s6, s10, 3\n" \
    "s_mul_i32 s7, s6, 37\n" \
    "s_lshr_b32 s7, s7, 8\n" \
    "s_mul_i32 s8, s7, 7\n" \
    "s_sub_u32 s8, s6, s8\n" \
    "s_cmp_ge_u32 s8, 3\n" \
    "s_addc_u32 s5, s8, 0\n" \
    "s_cmp_eq_u32 s7, 4\n" \
    "s_cselect_b32 s5, 3, s5\n" \
    "s_cselect_b32 s7, s8, s7\n" \
    "s_cselect_b32 s8, 3, s8\n" \
    "s_and_b32 s4, s10, 7\n" \
    "s_lshl2_add_u32 s4, s4, s7\n" \
    "s_lshl3_add_u32 s6, s4, s5\n" \
    "s_lshl2_add_u32 s6, s6, s31\n" \
    "s_lshl_b32 s6, s6, 12\n" \
    "s_add_u32 s35, s6, 12058624\n" \
    "s_mul_i32 s6, s4, 640\n" \
    "s_add_u32 s35, s35, s6\n" \
    "s_lshl_b32 s6, s4, 16\n" \
    "s_lshl_b32 s7, s5, 7\n" \
    "s_add_u32 s68, s6, s7\n" \
    "v_lshl_add_u32 v218, v6, 4, s7\n" \
    "s_lshr_b32 s6, s4, 1\n" \
    "s_lshl_b32 s6, s6, 13\n" \
    "s_and_b32 s7, s4, 1\n" \
    "s_lshl_b32 s7, s7, 7\n" \
    "s_add_u32 s6, s6, s7\n" \
    "s_lshl_b32 s7, s30, 8\n" \
    "s_add_u32 s6, s6, s7\n" \
    "s_lshr_b32 s7, s31, 1\n" \
    "s_lshl_b32 s7, s7, 4\n" \
    "s_add_u32 s6, s6, s7\n" \
    "s_add_u32 s37, s6, 16515072\n" \
    "v_bfe_u32 v3, v5, 1, 1\n" \
    "v_lshl_add_u32 v3, v6, 1, v3\n" \
    "v_lshrrev_b32 v4, 2, v5\n" \
    "v_lshl_add_u32 v3, v3, 3, v4\n" \
    "v_and_b32 v4, 1, v5\n" \
    "v_lshl_add_u32 v3, v3, 3, v4\n" \
    "v_lshlrev_b32 v217, 3, v3\n" \
    "s_lshl_b32 s6, s31, 1\n" \
    "v_lshrrev_b32 v3, 1, v2\n" \
    "v_xor_b32 v3, s6, v3\n" \
    "v_lshl_add_u32 v3, v1, 3, v3\n" \
    "v_and_b32 v4, 1, v2\n" \
    "v_lshl_add_u32 v3, v3, 1, v4\n" \
    "s_lshl_b32 s6, s31, 11\n" \
    "s_add_u32 s6, s6, 131072\n" \
    "v_lshl_add_u32 v219, v3, 3, s6\n" \
    "v_xor_b32 v3, 16, v219\n" \
    "v_add_u32 v220, 1024, v3\n" \
    "v_xor_b32 v3, s31, v6\n" \
    "v_lshl_add_u32 v3, v6, 6, v3\n" \
    "v_lshl_add_u32 v3, v3, 3, v5\n" \
    "s_mov_b32 s6, 131072\n" \
    "v_lshl_add_u32 v221, v3, 1, s6\n" \
    "v_xor_b32 v222, 64, v221\n" \
    "v_bfe_u32 v3, v5, 1, 1\n" \
    "v_and_b32 v4, 1, v5\n" \
    "v_lshl_or_b32 v3, v4, 1, v3\n" \
    "v_and_or_b32 v3, v5, 4, v3\n" \
    "v_and_b32 v4, 3, v6\n" \
    "v_lshlrev_b32 v4, 1, v4\n" \
    "v_xor_b32 v3, v3, v4\n" \
    "v_lshl_add_u32 v3, v5, 3, v3\n" \
    "v_lshrrev_b32 v4, 2, v6\n" \
    "v_lshl_add_u32 v3, v4, 8, v3\n" \
    "s_lshl_b32 s6, s31, 10\n" \
    "s_add_u32 s6, s6, 65536\n" \
    "v_lshl_add_u32 v223, v3, 4, s6\n" \
    "v_xor_b32 v224, 16, v223\n" \
    "v_lshlrev_b32 v3, 4, v6\n" \
    "v_lshl_add_u32 v225, v5, 10, v3\n" \
    "s_lshl_b32 s6, s31, 13\n" \
    "s_add_u32 s68, s68, s6\n" \
    "s_waitcnt vmcnt(0)\n" \
    "v_mov_b32 v241, 2\n" \
    "ds_write_b32 v226, v241 offset:80\n" \
    "Lp41:\n" \
    "ds_read_b32 v240, v227 offset:80\n" \
    "s_sub_u32 s70, s70, 1\n" \
    "s_cbranch_scc1 Labort\n" \
    "s_waitcnt lgkmcnt(0)\n" \
    "v_cmp_gt_u32 vcc, 2, v240\n" \
    "s_cbranch_vccnz Lp41\n" \
    "v_add_u32 v199, s28, v211\n" \
    "s_cmp_eq_u32 s30, 0\n" \
    "s_cbranch_scc0 Lconv1\n" \
    "ds_read2_b32 v[32:33], v233 offset0:112 offset1:113\n" \
    "ds_read2_b32 v[34:35], v233 offset0:114 offset1:115\n" \
    "ds_read_b128 v[92:95], v214 offset:4032\n" \
    "ds_read_b128 v[88:91], v214 offset:3520\n" \
    "ds_read2_b32 v[36:37], v234 offset0:112 offset1:113\n" \
    "ds_read2_b32 v[38:39], v234 offset0:114 offset1:115\n" \
    "ds_read_b128 v[84:87], v214 offset:3008\n" \
    "ds_read2_b32 v[40:41], v235 offset0:112 offset1:113\n" \
    "ds_read2_b32 v[42:43], v235 offset0:114 offset1:115\n" \
    "ds_read_b128 v[80:83], v214 offset:2496\n" \
    "ds_read2_b32 v[44:45], v236 offset0:112 offset1:113\n" \
    "ds_read2_b32 v[46:47], v236 offset0:114 offset1:115\n" \
    "ds_read_b128 v[76:79], v214 offset:1984\n" \
    "ds_read2_b32 v[48:49], v237 offset0:112 offset1:113\n" \
    "ds_read2_b32 v[50:51], v237 offset0:114 offset1:115\n" \
    "ds_read_b128 v[72:75], v214 offset:1472\n" \
    "ds_read2_b32 v[52:53], v248 offset0:112 offset1:113\n" \
    "ds_read2_b32 v[54:55], v248 offset0:114 offset1:115\n" \
    "ds_read_b128 v[68:71], v214 offset:960\n" \
    "ds_read2_b32 v[56:57], v249 offset0:112 offset1:113\n" \
    "ds_read2_b32 v[58:59], v249 offset0:114 offset1:115\n" \
    "ds_read_b128 v[64:67], v214 offset:448\n" \
    "ds_read2_b32 v[60:61], v250 offset0:112 offset1:113\n" \
    "ds_read2_b32 v[62:63], v250 offset0:114 offset1:115\n" \
    "s_waitcnt lgkmcnt(15)\n" \
    "v_mfma_f32_16x16x32_bf16 v[172:175], v[32:35], v[92:95], 0\n" \
    "v_mfma_f32_16x16x32_bf16 v[188:191], v[32:35], v[88:91], 0\n" \
    "ds_read2_b32 v[96:97], v233 offset0:96 offset1:97\n" \
    "v_mfma_f32_16x16x32_bf16 v[172:175], v[36:39], v[88:91], v[172:175]\n" \
    "ds_read2_b32 v[98:99], v233 offset0:98 offset1:99\n" \
    "v_mfma_f32_16x16x32_bf16 v[184:187], v[32:35], v[84:87], 0\n" \
    "ds_read_b128 v[156:159], v214 offset:3968\n" \
    "v_mfma_f32_16x16x32_bf16 v[172:175], v[40:43], v[84:87], v[172:175]\n" \
    "ds_read_b128 v[152:155], v214 offset:3456\n" \
    "v_mfma_f32_16x16x32_bf16 v[188:191], v[36:39], v[84:87], v[188:191]\n" \
    "ds_read2_b32 v[100:101], v234 offset0:96 offset1:97\n" \
    "s_waitcnt lgkmcnt(15)\n" \
    "v_mfma_f32_16x16x32_bf16 v[168:171], v[32:35], v[80:83], 0\n" \
    "ds_read2_b32 v[102:103], v234 offset0:98 offset1:99\n" \
    "v_mfma_f32_16x16x32_bf16 v[172:175], v[44:47], v[80:83], v[172:175]\n" \
    "ds_read_b128 v[148:151], v214 offset:2944\n" \
    "v_mfma_f32_16x16x32_bf16 v[184:187], v[36:39], v[80:83], v[184:187]\n" \
    "ds_read2_b32 v[104:105], v235 offset0:96 offset1:97\n" \
    "v_mfma_f32_16x16x32_bf16 v[188:191], v[40:43], v[80:83], v[188:191]\n" \
    "ds_read2_b32 v[106:107], v235 offset0:98 offset1:99\n" \
    "v_mfma_f32_16x16x32_bf16 v[164:167], v[32:35], v[76:79], 0\n" \
    "ds_read_b128 v[144:147], v214 offset:2432\n" \
    "v_mfma_f32_16x16x32_bf16 v[168:171], v[36:39], v[76:79], v[168:171]\n" \
    "ds_read2_b32 v[108:109], v236 offset0:96 offset1:97\n" \
    "s_waitcnt lgkmcnt(15)\n" \
    "v_mfma_f32_16x16x32_bf16 v[172:175], v[48:51], v[76:79], v[172:175]\n" \
    "ds_read2_b32 v[110:111], v236 offset0:98 offset1:99\n" \
    "v_mfma_f32_16x16x32_bf16 v[184:187], v[40:43], v[76:79], v[184:187]\n" \
    "v_mfma_f32_16x16x32_bf16 v[188:191], v[44:47], v[76:79], v[188:191]\n" \
    "ds_read_b128 v[140:143], v214 offset:1920\n" \
    "v_mfma_f32_16x16x32_bf16 v[164:167], v[36:39], v[72:75], v[164:167]\n" \
    "ds_read2_b32 v[112:113], v237 offset0:96 offset1:97\n" \
    "v_mfma_f32_16x16x32_bf16 v[168:171], v[40:43], v[72:75], v[168:171]\n" \
    "ds_read2_b32 v[114:115], v237 offset0:98 offset1:99\n" \
    "v_mfma_f32_16x16x32_bf16 v[172:175], v[52:55], v[72:75], v[172:175]\n" \
    "ds_read_b128 v[136:139], v214 offset:1408\n" \
    "v_mfma_f32_16x16x32_bf16 v[180:183], v[32:35], v[72:75], 0\n" \
    "ds_read2_b32 v[116:117], v248 offset0:96 offset1:97\n" \
    "v_mfma_f32_16x16x32_bf16 v[184:187], v[44:47], v[72:75], v[184:187]\n" \
    "ds_read2_b32 v[118:119], v248 offset0:98 offset1:99\n" \
    "v_mfma_f32_16x16x32_bf16 v[188:191], v[48:51], v[72:75], v[188:191]\n" \
    "ds_read_b128 v[132:135], v214 offset:896\n" \
    "v_mfma_f32_16x16x32_bf16 v[164:167], v[40:43], v[68:71], v[164:167]\n" \
    "ds_read2_b32 v[120:121], v249 offset0:96 offset1:97\n" \
    "v_mfma_f32_16x16x32_bf16 v[168:171], v[44:47], v[68:71], v[168:171]\n" \
    "ds_read2_b32 v[122:123], v249 offset0:98 offset1:99\n" \
    "s_waitcnt lgkmcnt(15)\n" \
    "v_mfma_f32_16x16x32_bf16 v[172:175], v[56:59], v[68:71], v[172:175]\n" \
    "ds_read_b128 v[128:131], v214 offset:384\n" \
    "v_mfma_f32_16x16x32_bf16 v[176:179], v[32:35], v[68:71], 0\n" \
    "ds_read2_b32 v[124:125], v250 offset0:96 offset1:97\n" \
    "v_mfma_f32_16x16x32_bf16 v[180:183], v[36:39], v[68:71], v[180:183]\n" \
    "ds_read2_b32 v[126:127], v250 offset0:98 offset1:99\n" \
    "v_mfma_f32_16x16x32_bf16 v[184:187], v[48:51], v[68:71], v[184:187]\n" \
    "v_mfma_f32_16x16x32_bf16 v[188:191], v[52:55], v[68:71], v[188:191]\n" \
    "v_mfma_f32_16x16x32_bf16 v[160:163], v[32:35], v[64:67], 0\n" \
    "v_mfma_f32_16x16x32_bf16 v[164:167], v[44:47], v[64:67], v[164:167]\n" \
    "v_mfma_f32_16x16x32_bf16 v[168:171], v[48:51], v[64:67], v[168:171]\n" \
    "v_mfma_f32_16x16x32_bf16 v[172:175], v[60:63], v[64:67], v[172:175]\n" \
    "v_mfma_f32_16x16x32_bf16 v[176:179], v[36:39], v[64:67], v[176:179]\n" \
    "v_mfma_f32_16x16x32_bf16 v[180:183], v[40:43], v[64:67], v[180:183]\n" \
    "v_mfma_f32_16x16x32_bf16 v[184:187], v[52:55], v[64:67], v[184:187]\n" \
    "v_mfma_f32_16x16x32_bf16 v[188:191], v[56:59], v[64:67], v[188:191]\n" \
    "v_mfma_f32_16x16x32_bf16 v[172:175], v[96:99], v[156:159], v[172:175]\n" \
    "v_mfma_f32_16x16x32_bf16 v[188:191], v[96:99], v[152:155], v[188:191]\n" \
    "ds_read2_b32 v[32:33], v233 offset0:80 offset1:81\n" \
    "v_mfma_f32_16x16x32_bf16 v[172:175], v[100:103], v[152:155], v[172:175]\n" \
    "ds_read2_b32 v[34:35], v233 offset0:82 offset1:83\n" \
    "s_waitcnt lgkmcnt(15)\n" \
    "v_mfma_f32_16x16x32_bf16 v[184:187], v[96:99], v[148:151], v[184:187]\n" \
    "ds_read_b128 v[92:95], v214 offset:3904\n" \
    "v_mfma_f32_16x16x32_bf16 v[172:175], v[104:107], v[148:151], v[172:175]\n" \
    "ds_read_b128 v[88:91], v214 offset:3392\n" \
    "v_mfma_f32_16x16x32_bf16 v[188:191], v[100:103], v[148:151], v[188:191]\n" \
    "ds_read2_b32 v[36:37], v234 offset0:80 offset1:81\n" \
    "v_mfma_f32_16x16x32_bf16 v[168:171], v[96:99], v[144:147], v[168:171]\n" \
    "ds_read2_b32 v[38:39], v234 offset0:82 offset1:83\n" \
    "s_waitcnt lgkmcnt(15)\n" \
    "v_mfma_f32_16x16x32_bf16 v[172:175], v[108:111], v[144:147], v[172:175]\n" \
    "ds_read_b128 v[84:87], v214 offset:2880\n" \
    "v_mfma_f32_16x16x32_bf16 v[184:187], v[100:103], v[144:147], v[184:187]\n" \
    "ds_read2_b32 v[40:41], v235 offset0:80 offset1:81\n" \
    "v_mfma_f32_16x16x32_bf16 v[188:191], v[104:107], v[144:147], v[188:191]\n" \
    "ds_read2_b32 v[42:43], v235 offset0:82 offset1:83\n" \
    "v_mfma_f32_16x16x32_bf16 v[164:167], v[96:99], v[140:143], v[164:167]\n" \
    "ds_read_b128 v[80:83], v214 offset:2368\n" \
    "v_mfma_f32_16x16x32_bf16 v[168:171], v[100:103], v[140:143], v[168:171]\n" \
    "ds_read2_b32 v[44:45], v236 offset0:80 offset1:81\n" \
    "v_mfma_f32_16x16x32_bf16 v[172:175], v[112:115], v[140:143], v[172:175]\n" \
    "ds_read2_b32 v[46:47], v236 offset0:82 offset1:83\n" \
    "v_mfma_f32_16x16x32_bf16 v[184:187], v[104:107], v[140:143], v[184:187]\n" \
    "v_mfma_f32_16x16x32_bf16 v[188:191], v[108:111], v[140:143], v[188:191]\n" \
    "ds_read_b128 v[76:79], v214 offset:1856\n" \
    "s_waitcnt lgkmcnt(15)\n" \
    "v_mfma_f32_16x16x32_bf16 v[164:167], v[100:103], v[136:139], v[164:167]\n" \
    "ds_read2_b32 v[48:49], v237 offset0:80 offset1:81\n" \
    "v_mfma_f32_16x16x32_bf16 v[168:171], v[104:107], v[136:139], v[168:171]\n" \
    "ds_read2_b32 v[50:51], v237 offset0:82 offset1:83\n" \
    "v_mfma_f32_16x16x32_bf16 v[172:175], v[116:119], v[136:139], v[172:175]\n" \
    "ds_read_b128 v[72:75], v214 offset:1344\n" \
    "v_mfma_f32_16x16x32_bf16 v[180:183], v[96:99], v[136:139], v[180:183]\n" \
    "ds_read2_b32 v[52:53], v248 offset0:80 offset1:81\n" \
    "v_mfma_f32_16x16x32_bf16 v[184:187], v[108:111], v[136:139], v[184:187]\n" \
    "ds_read2_b32 v[54:55], v248 offset0:82 offset1:83\n" \
    "v_mfma_f32_16x16x32_bf16 v[188:191], v[112:115], v[136:139], v[188:191]\n" \
    "ds_read_b128 v[68:71], v214 offset:832\n" \
    "v_mfma_f32_16x16x32_bf16 v[164:167], v[104:107], v[132:135], v[164:167]\n" \
    "ds_read2_b32 v[56:57], v249 offset0:80 offset1:81\n" \
    "v_mfma_f32_16x16x32_bf16 v[168:171], v[108:111], v[132:135], v[168:171]\n" \
    "ds_read2_b32 v[58:59], v249 offset0:82 offset1:83\n" \
    "v_mfma_f32_16x16x32_bf16 v[172:175], v[120:123], v[132:135], v[172:175]\n" \
    "ds_read_b128 v[64:67], v214 offset:320\n" \
    "v_mfma_f32_16x16x32_bf16 v[176:179], v[96:99], v[132:135], v[176:179]\n" \
    "ds_read2_b32 v[60:61], v250 offset0:80 offset1:81\n" \
    "v_mfma_f32_16x16x32_bf16 v[180:183], v[100:103], v[132:135], v[180:183]\n" \
    "ds_read2_b32 v[62:63], v250 offset0:82 offset1:83\n" \
    "v_mfma_f32_16x16x32_bf16 v[184:187], v[112:115], v[132:135], v[184:187]\n" \
    "v_mfma_f32_16x16x32_bf16 v[188:191], v[116:119], v[132:135], v[188:191]\n" \
    "v_mfma_f32_16x16x32_bf16 v[160:163], v[96:99], v[128:131], v[160:163]\n" \
    "v_mfma_f32_16x16x32_bf16 v[164:167], v[108:111], v[128:131], v[164:167]\n" \
    "v_mfma_f32_16x16x32_bf16 v[168:171], v[112:115], v[128:131], v[168:171]\n" \
    "s_waitcnt lgkmcnt(15)\n" \
    "v_mfma_f32_16x16x32_bf16 v[172:175], v[124:127], v[128:131], v[172:175]\n" \
    "v_mfma_f32_16x16x32_bf16 v[176:179], v[100:103], v[128:131], v[176:179]\n" \
    "v_mfma_f32_16x16x32_bf16 v[180:183], v[104:107], v[128:131], v[180:183]\n" \
    "v_mfma_f32_16x16x32_bf16 v[184:187], v[116:119], v[128:131], v[184:187]\n" \
    "v_mfma_f32_16x16x32_bf16 v[188:191], v[120:123], v[128:131], v[188:191]\n" \
    "v_mfma_f32_16x16x32_bf16 v[172:175], v[32:35], v[92:95], v[172:175]\n" \
    "v_mfma_f32_16x16x32_bf16 v[188:191], v[32:35], v[88:91], v[188:191]\n" \
    "ds_read2_b32 v[96:97], v233 offset0:64 offset1:65\n" \
    "v_mfma_f32_16x16x32_bf16 v[172:175], v[36:39], v[88:91], v[172:175]\n" \
    "ds_read2_b32 v[98:99], v233 offset0:66 offset1:67\n" \
    "v_mfma_f32_16x16x32_bf16 v[184:187], v[32:35], v[84:87], v[184:187]\n" \
    "ds_read_b128 v[152:155], v214 offset:3328\n" \
    "v_mfma_f32_16x16x32_bf16 v[172:175], v[40:43], v[84:87], v[172:175]\n" \
    "ds_read_b128 v[148:151], v214 offset:2816\n" \
    "v_mfma_f32_16x16x32_bf16 v[188:191], v[36:39], v[84:87], v[188:191]\n" \
    "ds_read2_b32 v[100:101], v234 offset0:64 offset1:65\n" \
    "s_waitcnt lgkmcnt(15)\n" \
    "v_mfma_f32_16x16x32_bf16 v[168:171], v[32:35], v[80:83], v[168:171]\n" \
    "ds_read2_b32 v[102:103], v234 offset0:66 offset1:67\n" \
    "v_mfma_f32_16x16x32_bf16 v[172:175], v[44:47], v[80:83], v[172:175]\n" \
    "ds_read_b128 v[144:147], v214 offset:2304\n" \
    "v_mfma_f32_16x16x32_bf16 v[184:187], v[36:39], v[80:83], v[184:187]\n" \
    "ds_read2_b32 v[104:105], v235 offset0:64 offset1:65\n" \
    "v_mfma_f32_16x16x32_bf16 v[188:191], v[40:43], v[80:83], v[188:191]\n" \
    "ds_read2_b32 v[106:107], v235 offset0:66 offset1:67\n" \
    "v_mfma_f32_16x16x32_bf16 v[164:167], v[32:35], v[76:79], v[164:167]\n" \
    "ds_read_b128 v[140:143], v214 offset:1792\n" \
    "v_mfma_f32_16x16x32_bf16 v[168:171], v[36:39], v[76:79], v[168:171]\n" \
    "ds_read2_b32 v[108:109], v236 offset0:64 offset1:65\n" \
    "s_waitcnt lgkmcnt(15)\n" \
    "v_mfma_f32_16x16x32_bf16 v[172:175], v[48:51], v[76:79], v[172:175]\n" \
    "ds_read2_b32 v[110:111], v236 offset0:66 offset1:67\n" \
    "v_mfma_f32_16x16x32_bf16 v[184:187], v[40:43], v[76:79], v[184:187]\n" \
    "v_mfma_f32_16x16x32_bf16 v[188:191], v[44:47], v[76:79], v[188:191]\n" \
    "ds_read_b128 v[136:139], v214 offset:1280\n" \
    "v_mfma_f32_16x16x32_bf16 v[164:167], v[36:39], v[72:75], v[164:167]\n" \
    "ds_read2_b32 v[112:113], v237 offset0:64 offset1:65\n" \
    "v_mfma_f32_16x16x32_bf16 v[168:171], v[40:43], v[72:75], v[168:171]\n" \
    "ds_read2_b32 v[114:115], v237 offset0:66 offset1:67\n" \
    "v_mfma_f32_16x16x32_bf16 v[172:175], v[52:55], v[72:75], v[172:175]\n" \
    "ds_read_b128 v[132:135], v214 offset:768\n" \
    "v_mfma_f32_16x16x32_bf16 v[180:183], v[32:35], v[72:75], v[180:183]\n" \
    "ds_read2_b32 v[116:117], v248 offset0:64 offset1:65\n" \
    "v_mfma_f32_16x16x32_bf16 v[184:187], v[44:47], v[72:75], v[184:187]\n" \
    "ds_read2_b32 v[118:119], v248 offset0:66 offset1:67\n" \
    "v_mfma_f32_16x16x32_bf16 v[188:191], v[48:51], v[72:75], v[188:191]\n" \
    "ds_read_b128 v[128:131], v214 offset:256\n" \
    "v_mfma_f32_16x16x32_bf16 v[164:167], v[40:43], v[68:71], v[164:167]\n" \
    "ds_read2_b32 v[120:121], v249 offset0:64 offset1:65\n" \
    "v_mfma_f32_16x16x32_bf16 v[168:171], v[44:47], v[68:71], v[168:171]\n" \
    "ds_read2_b32 v[122:123], v249 offset0:66 offset1:67\n" \
    "s_waitcnt lgkmcnt(15)\n" \
    "v_mfma_f32_16x16x32_bf16 v[172:175], v[56:59], v[68:71], v[172:175]\n" \
    "ds_read_b128 v[156:159], v214 offset:3840\n" \
    "v_mfma_f32_16x16x32_bf16 v[176:179], v[32:35], v[68:71], v[176:179]\n" \
    "ds_read2_b32 v[124:125], v250 offset0:64 offset1:65\n" \
    "v_mfma_f32_16x16x32_bf16 v[180:183], v[36:39], v[68:71], v[180:183]\n" \
    "ds_read2_b32 v[126:127], v250 offset0:66 offset1:67\n" \
    "v_mov_b32 v241, 3\n" \
    "ds_write_b32 v226, v241 offset:80\n" \
    "v_mfma_f32_16x16x32_bf16 v[184:187], v[48:51], v[68:71], v[184:187]\n" \
    "v_mfma_f32_16x16x32_bf16 v[188:191], v[52:55], v[68:71], v[188:191]\n" \
    "v_mfma_f32_16x16x32_bf16 v[160:163], v[32:35], v[64:67], v[160:163]\n" \
    "v_mfma_f32_16x16x32_bf16 v[164:167], v[44:47], v[64:67], v[164:167]\n" \
    "v_mfma_f32_16x16x32_bf16 v[168:171], v[48:51], v[64:67], v[168:171]\n" \
    "v_mfma_f32_16x16x32_bf16 v[172:175], v[60:63], v[64:67], v[172:175]\n" \
    "v_mfma_f32_16x16x32_bf16 v[176:179], v[36:39], v[64:67], v[176:179]\n" \
    "v_mfma_f32_16x16x32_bf16 v[180:183], v[40:43], v[64:67], v[180:183]\n" \
    "v_mfma_f32_16x16x32_bf16 v[184:187], v[52:55], v[64:67], v[184:187]\n" \
    "v_mfma_f32_16x16x32_bf16 v[188:191], v[56:59], v[64:67], v[188:191]\n" \
    "v_mfma_f32_16x16x32_bf16 v[188:191], v[96:99], v[152:155], v[188:191]\n" \
    "v_mfma_f32_16x16x32_bf16 v[184:187], v[96:99], v[148:151], v[184:187]\n" \
    "v_mfma_f32_16x16x32_bf16 v[188:191], v[100:103], v[148:151], v[188:191]\n" \
    "s_waitcnt lgkmcnt(15)\n" \
    "v_mfma_f32_16x16x32_bf16 v[184:187], v[100:103], v[144:147], v[184:187]\n" \
    "v_mfma_f32_16x16x32_bf16 v[188:191], v[104:107], v[144:147], v[188:191]\n" \
    "v_mfma_f32_16x16x32_bf16 v[184:187], v[104:107], v[140:143], v[184:187]\n" \
    "s_waitcnt lgkmcnt(13)\n" \
    "v_mfma_f32_16x16x32_bf16 v[188:191], v[108:111], v[140:143], v[188:191]\n" \
    "s_waitcnt lgkmcnt(12)\n" \
    "v_mfma_f32_16x16x32_bf16 v[180:183], v[96:99], v[136:139], v[180:183]\n" \
    "v_mfma_f32_16x16x32_bf16 v[184:187], v[108:111], v[136:139], v[184:187]\n" \
    "s_waitcnt lgkmcnt(10)\n" \
    "v_mfma_f32_16x16x32_bf16 v[188:191], v[112:115], v[136:139], v[188:191]\n" \
    "s_waitcnt lgkmcnt(9)\n" \
    "v_mfma_f32_16x16x32_bf16 v[176:179], v[96:99], v[132:135], v[176:179]\n" \
    "v_mfma_f32_16x16x32_bf16 v[180:183], v[100:103], v[132:135], v[180:183]\n" \
    "v_mfma_f32_16x16x32_bf16 v[184:187], v[112:115], v[132:135], v[184:187]\n" \
    "s_waitcnt lgkmcnt(7)\n" \
    "v_mfma_f32_16x16x32_bf16 v[188:191], v[116:119], v[132:135], v[188:191]\n" \
    "s_waitcnt lgkmcnt(6)\n" \
    "v_mfma_f32_16x16x32_bf16 v[176:179], v[100:103], v[128:131], v[176:179]\n" \
    "v_mfma_f32_16x16x32_bf16 v[180:183], v[104:107], v[128:131], v[180:183]\n" \
    "v_mfma_f32_16x16x32_bf16 v[184:187], v[116:119], v[128:131], v[184:187]\n" \
    "s_waitcnt lgkmcnt(4)\n" \
    "v_mfma_f32_16x16x32_bf16 v[188:191], v[120:123], v[128:131], v[188:191]\n" \
    "s_waitcnt lgkmcnt(3)\n" \
    "v_mfma_f32_16x16x32_bf16 v[172:175], v[96:99], v[156:159], v[172:175]\n" \
    "v_mfma_f32_16x16x32_bf16 v[168:171], v[96:99], v[144:147], v[168:171]\n" \
    "Lp42:\n" \
    "ds_read_b32 v240, v227 offset:80\n" \
    "s_sub_u32 s70, s70, 1\n" \
    "s_cbranch_scc1 Labort\n" \
    "s_waitcnt lgkmcnt(0)\n" \
    "v_cmp_gt_u32 vcc, 3, v240\n" \
    "s_cbranch_vccnz Lp42\n" \
    "ds_write_b128 v199, v[176:179] offset:0\n" \
    "ds_write_b128 v199, v[180:183] offset:1024\n" \
    "ds_write_b128 v199, v[184:187] offset:2048\n" \
    "ds_write_b128 v199, v[188:191] offset:3072\n" \
    "v_mov_b32 v241, 4\n" \
    "ds_write_b32 v226, v241 offset:80\n" \
    "v_mfma_f32_16x16x32_bf16 v[172:175], v[100:103], v[152:155], v[172:175]\n" \
    "v_mfma_f32_16x16x32_bf16 v[164:167], v[96:99], v[140:143], v[164:167]\n" \
    "v_mfma_f32_16x16x32_bf16 v[172:175], v[104:107], v[148:151], v[172:175]\n" \
    "v_mfma_f32_16x16x32_bf16 v[168:171], v[100:103], v[140:143], v[168:171]\n" \
    "v_mfma_f32_16x16x32_bf16 v[172:175], v[108:111], v[144:147], v[172:175]\n" \
    "v_mfma_f32_16x16x32_bf16 v[164:167], v[100:103], v[136:139], v[164:167]\n" \
    "v_mfma_f32_16x16x32_bf16 v[172:175], v[112:115], v[140:143], v[172:175]\n" \
    "v_mfma_f32_16x16x32_bf16 v[168:171], v[104:107], v[136:139], v[168:171]\n" \
    "v_mfma_f32_16x16x32_bf16 v[172:175], v[116:119], v[136:139], v[172:175]\n" \
    "v_mfma_f32_16x16x32_bf16 v[164:167], v[104:107], v[132:135], v[164:167]\n" \
    "v_mfma_f32_16x16x32_bf16 v[168:171], v[108:111], v[132:135], v[168:171]\n" \
    "v_mfma_f32_16x16x32_bf16 v[172:175], v[120:123], v[132:135], v[172:175]\n" \
    "v_mfma_f32_16x16x32_bf16 v[160:163], v[96:99], v[128:131], v[160:163]\n" \
    "v_mfma_f32_16x16x32_bf16 v[164:167], v[108:111], v[128:131], v[164:167]\n" \
    "v_mfma_f32_16x16x32_bf16 v[168:171], v[112:115], v[128:131], v[168:171]\n" \
    "v_mfma_f32_16x16x32_bf16 v[172:175], v[124:127], v[128:131], v[172:175]\n" \
    "Lp43:\n" \
    "ds_read_b32 v240, v227 offset:80\n" \
    "s_sub_u32 s70, s70, 1\n" \
    "s_cbranch_scc1 Labort\n" \
    "s_waitcnt lgkmcnt(0)\n" \
    "v_cmp_gt_u32 vcc, 4, v240\n" \
    "s_cbranch_vccnz Lp43\n" \
    "ds_read_b128 v[48:51], v199 offset:4096\n" \
    "ds_read_b128 v[52:55], v199 offset:5120\n" \
    "ds_read_b128 v[56:59], v199 offset:6144\n" \
    "ds_read_b128 v[60:63], v199 offset:7168\n" \
    "v_mov_b32 v241, 6\n" \
    "ds_write_b32 v226, v241 offset:80\n" \
    "v_mov_b32 v17, v244\n" \
    "v_mov_b32 v19, v244\n" \
    "v_mov_b32 v21, v244\n" \
    "v_mov_b32 v23, v244\n" \
    "v_mov_b32 v25, v244\n" \
    "v_mov_b32 v27, v244\n" \
    "v_mov_b32 v29, v244\n" \
    "v_mov_b32 v31, v244\n" \
    "s_waitcnt lgkmcnt(4)\n" \
    "v_add_f32 v160, v160, v48\n" \
    "v_add_f32 v161, v161, v49\n" \
    "v_add_f32 v162, v162, v50\n" \
    "v_add_f32 v163, v163, v51\n" \
    "v_cvt_pk_bf16_f32 v32, v160, v161\n" \
    "v_cvt_pk_bf16_f32 v33, v162, v163\n" \
    "ds_bpermute_b32 v16, v215, v32\n" \
    "ds_bpermute_b32 v18, v215, v33\n" \
    "s_waitcnt lgkmcnt(5)\n" \
    "v_add_f32 v164, v164, v52\n" \
    "v_add_f32 v165, v165, v53\n" \
    "v_add_f32 v166, v166, v54\n" \
    "v_add_f32 v167, v167, v55\n" \
    "v_cvt_pk_bf16_f32 v34, v164, v165\n" \
    "v_cvt_pk_bf16_f32 v35, v166, v167\n" \
    "ds_bpermute_b32 v20, v215, v34\n" \
    "ds_bpermute_b32 v22, v215, v35\n" \
    "s_waitcnt lgkmcnt(6)\n" \
    "v_add_f32 v168, v168, v56\n" \
    "v_add_f32 v169, v169, v57\n" \
    "v_add_f32 v170, v170, v58\n" \
    "v_add_f32 v171, v171, v59\n" \
    "v_cvt_pk_bf16_f32 v36, v168, v169\n" \
    "v_cvt_pk_bf16_f32 v37, v170, v171\n" \
    "ds_bpermute_b32 v24, v215, v36\n" \
    "ds_bpermute_b32 v26, v215, v37\n" \
    "s_waitcnt lgkmcnt(7)\n" \
    "v_add_f32 v172, v172, v60\n" \
    "v_add_f32 v173, v173, v61\n" \
    "v_add_f32 v174, v174, v62\n" \
    "v_add_f32 v175, v175, v63\n" \
    "v_cvt_pk_bf16_f32 v38, v172, v173\n" \
    "v_cvt_pk_bf16_f32 v39, v174, v175\n" \
    "ds_bpermute_b32 v28, v215, v38\n" \
    "ds_bpermute_b32 v30, v215, v39\n" \
    "s_setprio 3\n" \
    "s_add_u32 s4, s34, 0\n" \
    "s_waitcnt lgkmcnt(6)\n" \
    "buffer_store_dwordx4 v[16:19], v216, s[24:27], s4 offen sc1\n" \
    "s_add_u32 s4, s34, 1580544\n" \
    "s_waitcnt lgkmcnt(4)\n" \
    "buffer_store_dwordx4 v[20:23], v216, s[24:27], s4 offen sc1\n" \
    "s_add_u32 s4, s34, 2107392\n" \
    "s_waitcnt lgkmcnt(2)\n" \
    "buffer_store_dwordx4 v[24:27], v216, s[24:27], s4 offen sc1\n" \
    "s_add_u32 s4, s34, 3687936\n" \
    "s_waitcnt lgkmcnt(0)\n" \
    "buffer_store_dwordx4 v[28:31], v216, s[24:27], s4 offen sc1\n" \
    "s_branch Lconv2\n" \
    "Lconv1:\n" \
    "ds_read2_b32 v[32:33], v233 offset0:48 offset1:49\n" \
    "ds_read2_b32 v[34:35], v233 offset0:50 offset1:51\n" \
    "ds_read_b128 v[92:95], v214 offset:3776\n" \
    "ds_read_b128 v[88:91], v214 offset:3264\n" \
    "ds_read2_b32 v[36:37], v234 offset0:48 offset1:49\n" \
    "ds_read2_b32 v[38:39], v234 offset0:50 offset1:51\n" \
    "ds_read_b128 v[84:87], v214 offset:2752\n" \
    "ds_read2_b32 v[40:41], v235 offset0:48 offset1:49\n" \
    "ds_read2_b32 v[42:43], v235 offset0:50 offset1:51\n" \
    "ds_read_b128 v[80:83], v214 offset:2240\n" \
    "ds_read2_b32 v[44:45], v236 offset0:48 offset1:49\n" \
    "ds_read2_b32 v[46:47], v236 offset0:50 offset1:51\n" \
    "ds_read_b128 v[76:79], v214 offset:1728\n" \
    "ds_read2_b32 v[48:49], v237 offset0:48 offset1:49\n" \
    "ds_read2_b32 v[50:51], v237 offset0:50 offset1:51\n" \
    "ds_read_b128 v[72:75], v214 offset:1216\n" \
    "ds_read2_b32 v[52:53], v248 offset0:48 offset1:49\n" \
    "ds_read2_b32 v[54:55], v248 offset0:50 offset1:51\n" \
    "ds_read_b128 v[68:71], v214 offset:704\n" \
    "ds_read2_b32 v[56:57], v249 offset0:48 offset1:49\n" \
    "ds_read2_b32 v[58:59], v249 offset0:50 offset1:51\n" \
    "ds_read_b128 v[64:67], v214 offset:192\n" \
    "ds_read2_b32 v[60:61], v250 offset0:48 offset1:49\n" \
    "ds_read2_b32 v[62:63], v250 offset0:50 offset1:51\n" \
    "s_waitcnt lgkmcnt(15)\n" \
    "v_mfma_f32_16x16x32_bf16 v[188:191], v[32:35], v[92:95], 0\n" \
    "v_mfma_f32_16x16x32_bf16 v[172:175], v[32:35], v[88:91], 0\n" \
    "ds_read2_b32 v[96:97], v233 offset0:32 offset1:33\n" \
    "v_mfma_f32_16x16x32_bf16 v[188:191], v[36:39], v[88:91], v[188:191]\n" \
    "ds_read2_b32 v[98:99], v233 offset0:34 offset1:35\n" \
    "v_mfma_f32_16x16x32_bf16 v[168:171], v[32:35], v[84:87], 0\n" \
    "ds_read_b128 v[156:159], v214 offset:3712\n" \
    "v_mfma_f32_16x16x32_bf16 v[172:175], v[36:39], v[84:87], v[172:175]\n" \
    "ds_read_b128 v[152:155], v214 offset:3200\n" \
    "v_mfma_f32_16x16x32_bf16 v[188:191], v[40:43], v[84:87], v[188:191]\n" \
    "ds_read2_b32 v[100:101], v234 offset0:32 offset1:33\n" \
    "s_waitcnt lgkmcnt(15)\n" \
    "v_mfma_f32_16x16x32_bf16 v[168:171], v[36:39], v[80:83], v[168:171]\n" \
    "ds_read2_b32 v[102:103], v234 offset0:34 offset1:35\n" \
    "v_mfma_f32_16x16x32_bf16 v[172:175], v[40:43], v[80:83], v[172:175]\n" \
    "ds_read_b128 v[148:151], v214 offset:2688\n" \
    "v_mfma_f32_16x16x32_bf16 v[184:187], v[32:35], v[80:83], 0\n" \
    "ds_read2_b32 v[104:105], v235 offset0:32 offset1:33\n" \
    "v_mfma_f32_16x16x32_bf16 v[188:191], v[44:47], v[80:83], v[188:191]\n" \
    "ds_read2_b32 v[106:107], v235 offset0:34 offset1:35\n" \
    "v_mfma_f32_16x16x32_bf16 v[168:171], v[40:43], v[76:79], v[168:171]\n" \
    "ds_read_b128 v[144:147], v214 offset:2176\n" \
    "v_mfma_f32_16x16x32_bf16 v[172:175], v[44:47], v[76:79], v[172:175]\n" \
    "ds_read2_b32 v[108:109], v236 offset0:32 offset1:33\n" \
    "v_mfma_f32_16x16x32_bf16 v[180:183], v[32:35], v[76:79], 0\n" \
    "ds_read2_b32 v[110:111], v236 offset0:34 offset1:35\n" \
    "v_mfma_f32_16x16x32_bf16 v[184:187], v[36:39], v[76:79], v[184:187]\n" \
    "s_waitcnt lgkmcnt(15)\n" \
    "v_mfma_f32_16x16x32_bf16 v[188:191], v[48:51], v[76:79], v[188:191]\n" \
    "ds_read_b128 v[140:143], v214 offset:1664\n" \
    "v_mfma_f32_16x16x32_bf16 v[164:167], v[32:35], v[72:75], 0\n" \
    "ds_read2_b32 v[112:113], v237 offset0:32 offset1:33\n" \
    "v_mfma_f32_16x16x32_bf16 v[168:171], v[44:47], v[72:75], v[168:171]\n" \
    "ds_read2_b32 v[114:115], v237 offset0:34 offset1:35\n" \
    "v_mfma_f32_16x16x32_bf16 v[172:175], v[48:51], v[72:75], v[172:175]\n" \
    "ds_read_b128 v[136:139], v214 offset:1152\n" \
    "v_mfma_f32_16x16x32_bf16 v[180:183], v[36:39], v[72:75], v[180:183]\n" \
    "ds_read2_b32 v[116:117], v248 offset0:32 offset1:33\n" \
    "v_mfma_f32_16x16x32_bf16 v[184:187], v[40:43], v[72:75], v[184:187]\n" \
    "ds_read2_b32 v[118:119], v248 offset0:34 offset1:35\n" \
    "v_mfma_f32_16x16x32_bf16 v[188:191], v[52:55], v[72:75], v[188:191]\n" \
    "ds_read_b128 v[132:135], v214 offset:640\n" \
    "v_mfma_f32_16x16x32_bf16 v[160:163], v[32:35], v[68:71], 0\n" \
    "ds_read2_b32 v[120:121], v249 offset0:32 offset1:33\n" \
    "v_mfma_f32_16x16x32_bf16 v[164:167], v[36:39], v[68:71], v[164:167]\n" \
    "ds_read2_b32 v[122:123], v249 offset0:34 offset1:35\n" \
    "v_mfma_f32_16x16x32_bf16 v[168:171], v[48:51], v[68:71], v[168:171]\n" \
    "ds_read_b128 v[128:131], v214 offset:128\n" \
    "v_mfma_f32_16x16x32_bf16 v[172:175], v[52:55], v[68:71], v[172:175]\n" \
    "ds_read2_b32 v[124:125], v250 offset0:32 offset1:33\n" \
    "v_mfma_f32_16x16x32_bf16 v[180:183], v[40:43], v[68:71], v[180:183]\n" \
    "ds_read2_b32 v[126:127], v250 offset0:34 offset1:35\n" \
    "v_mfma_f32_16x16x32_bf16 v[184:187], v[44:47], v[68:71], v[184:187]\n" \
    "v_mfma_f32_16x16x32_bf16 v[188:191], v[56:59], v[68:71], v[188:191]\n" \
    "s_waitcnt lgkmcnt(15)\n" \
    "v_mfma_f32_16x16x32_bf16 v[160:163], v[36:39], v[64:67], v[160:163]\n" \
    "v_mfma_f32_16x16x32_bf16 v[164:167], v[40:43], v[64:67], v[164:167]\n" \
    "v_mfma_f32_16x16x32_bf16 v[168:171], v[52:55], v[64:67], v[168:171]\n" \
    "v_mfma_f32_16x16x32_bf16 v[172:175], v[56:59], v[64:67], v[172:175]\n" \
    "v_mfma_f32_16x16x32_bf16 v[176:179], v[32:35], v[64:67], 0\n" \
    "v_mfma_f32_16x16x32_bf16 v[180:183], v[44:47], v[64:67], v[180:183]\n" \
    "v_mfma_f32_16x16x32_bf16 v[184:187], v[48:51], v[64:67], v[184:187]\n" \
    "v_mfma_f32_16x16x32_bf16 v[188:191], v[60:63], v[64:67], v[188:191]\n" \
    "v_mfma_f32_16x16x32_bf16 v[188:191], v[96:99], v[156:159], v[188:191]\n" \
    "v_mfma_f32_16x16x32_bf16 v[172:175], v[96:99], v[152:155], v[172:175]\n" \
    "ds_read2_b32 v[32:33], v233 offset0:16 offset1:17\n" \
    "v_mfma_f32_16x16x32_bf16 v[188:191], v[100:103], v[152:155], v[188:191]\n" \
    "ds_read2_b32 v[34:35], v233 offset0:18 offset1:19\n" \
    "v_mfma_f32_16x16x32_bf16 v[168:171], v[96:99], v[148:151], v[168:171]\n" \
    "ds_read_b128 v[92:95], v214 offset:3648\n" \
    "v_mfma_f32_16x16x32_bf16 v[172:175], v[100:103], v[148:151], v[172:175]\n" \
    "ds_read_b128 v[88:91], v214 offset:3136\n" \
    "v_mfma_f32_16x16x32_bf16 v[188:191], v[104:107], v[148:151], v[188:191]\n" \
    "ds_read2_b32 v[36:37], v234 offset0:16 offset1:17\n" \
    "s_waitcnt lgkmcnt(15)\n" \
    "v_mfma_f32_16x16x32_bf16 v[168:171], v[100:103], v[144:147], v[168:171]\n" \
    "ds_read2_b32 v[38:39], v234 offset0:18 offset1:19\n" \
    "v_mfma_f32_16x16x32_bf16 v[172:175], v[104:107], v[144:147], v[172:175]\n" \
    "ds_read_b128 v[84:87], v214 offset:2624\n" \
    "v_mfma_f32_16x16x32_bf16 v[184:187], v[96:99], v[144:147], v[184:187]\n" \
    "ds_read2_b32 v[40:41], v235 offset0:16 offset1:17\n" \
    "v_mfma_f32_16x16x32_bf16 v[188:191], v[108:111], v[144:147], v[188:191]\n" \
    "ds_read2_b32 v[42:43], v235 offset0:18 offset1:19\n" \
    "v_mfma_f32_16x16x32_bf16 v[168:171], v[104:107], v[140:143], v[168:171]\n" \
    "ds_read_b128 v[80:83], v214 offset:2112\n" \
    "v_mfma_f32_16x16x32_bf16 v[172:175], v[108:111], v[140:143], v[172:175]\n" \
    "ds_read2_b32 v[44:45], v236 offset0:16 offset1:17\n" \
    "v_mfma_f32_16x16x32_bf16 v[180:183], v[96:99], v[140:143], v[180:183]\n" \
    "ds_read2_b32 v[46:47], v236 offset0:18 offset1:19\n" \
    "v_mfma_f32_16x16x32_bf16 v[184:187], v[100:103], v[140:143], v[184:187]\n" \
    "s_waitcnt lgkmcnt(15)\n" \
    "v_mfma_f32_16x16x32_bf16 v[188:191], v[112:115], v[140:143], v[188:191]\n" \
    "ds_read_b128 v[76:79], v214 offset:1600\n" \
    "v_mfma_f32_16x16x32_bf16 v[164:167], v[96:99], v[136:139], v[164:167]\n" \
    "ds_read2_b32 v[48:49], v237 offset0:16 offset1:17\n" \
    "v_mfma_f32_16x16x32_bf16 v[168:171], v[108:111], v[136:139], v[168:171]\n" \
    "ds_read2_b32 v[50:51], v237 offset0:18 offset1:19\n" \
    "v_mfma_f32_16x16x32_bf16 v[172:175], v[112:115], v[136:139], v[172:175]\n" \
    "ds_read_b128 v[72:75], v214 offset:1088\n" \
    "v_mfma_f32_16x16x32_bf16 v[180:183], v[100:103], v[136:139], v[180:183]\n" \
    "ds_read2_b32 v[52:53], v248 offset0:16 offset1:17\n" \
    "v_mfma_f32_16x16x32_bf16 v[184:187], v[104:107], v[136:139], v[184:187]\n" \
    "ds_read2_b32 v[54:55], v248 offset0:18 offset1:19\n" \
    "v_mfma_f32_16x16x32_bf16 v[188:191], v[116:119], v[136:139], v[188:191]\n" \
    "ds_read_b128 v[68:71], v214 offset:576\n" \
    "v_mfma_f32_16x16x32_bf16 v[160:163], v[96:99], v[132:135], v[160:163]\n" \
    "ds_read2_b32 v[56:57], v249 offset0:16 offset1:17\n" \
    "v_mfma_f32_16x16x32_bf16 v[164:167], v[100:103], v[132:135], v[164:167]\n" \
    "ds_read2_b32 v[58:59], v249 offset0:18 offset1:19\n" \
    "v_mfma_f32_16x16x32_bf16 v[168:171], v[112:115], v[132:135], v[168:171]\n" \
    "ds_read_b128 v[64:67], v214 offset:64\n" \
    "v_mfma_f32_16x16x32_bf16 v[172:175], v[116:119], v[132:135], v[172:175]\n" \
    "ds_read2_b32 v[60:61], v250 offset0:16 offset1:17\n" \
    "v_mfma_f32_16x16x32_bf16 v[180:183], v[104:107], v[132:135], v[180:183]\n" \
    "ds_read2_b32 v[62:63], v250 offset0:18 offset1:19\n" \
    "v_mfma_f32_16x16x32_bf16 v[184:187], v[108:111], v[132:135], v[184:187]\n" \
    "v_mfma_f32_16x16x32_bf16 v[188:191], v[120:123], v[132:135], v[188:191]\n" \
    "s_waitcnt lgkmcnt(15)\n" \
    "v_mfma_f32_16x16x32_bf16 v[160:163], v[100:103], v[128:131], v[160:163]\n" \
    "v_mfma_f32_16x16x32_bf16 v[164:167], v[104:107], v[128:131], v[164:167]\n" \
    "v_mfma_f32_16x16x32_bf16 v[168:171], v[116:119], v[128:131], v[168:171]\n" \
    "v_mfma_f32_16x16x32_bf16 v[172:175], v[120:123], v[128:131], v[172:175]\n" \
    "v_mfma_f32_16x16x32_bf16 v[176:179], v[96:99], v[128:131], v[176:179]\n" \
    "v_mfma_f32_16x16x32_bf16 v[180:183], v[108:111], v[128:131], v[180:183]\n" \
    "v_mfma_f32_16x16x32_bf16 v[184:187], v[112:115], v[128:131], v[184:187]\n" \
    "v_mfma_f32_16x16x32_bf16 v[188:191], v[124:127], v[128:131], v[188:191]\n" \
    "v_mfma_f32_16x16x32_bf16 v[188:191], v[32:35], v[92:95], v[188:191]\n" \
    "v_mfma_f32_16x16x32_bf16 v[172:175], v[32:35], v[88:91], v[172:175]\n" \
    "ds_read2_b32 v[96:97], v233 offset0:0 offset1:1\n" \
    "v_mfma_f32_16x16x32_bf16 v[188:191], v[36:39], v[88:91], v[188:191]\n" \
    "ds_read2_b32 v[98:99], v233 offset0:2 offset1:3\n" \
    "v_mfma_f32_16x16x32_bf16 v[168:171], v[32:35], v[84:87], v[168:171]\n" \
    "ds_read_b128 v[156:159], v214 offset:3584\n" \
    "v_mfma_f32_16x16x32_bf16 v[172:175], v[36:39], v[84:87], v[172:175]\n" \
    "ds_read_b128 v[144:147], v214 offset:2048\n" \
    "v_mfma_f32_16x16x32_bf16 v[188:191], v[40:43], v[84:87], v[188:191]\n" \
    "ds_read2_b32 v[100:101], v234 offset0:0 offset1:1\n" \
    "s_waitcnt lgkmcnt(15)\n" \
    "v_mfma_f32_16x16x32_bf16 v[168:171], v[36:39], v[80:83], v[168:171]\n" \
    "ds_read2_b32 v[102:103], v234 offset0:2 offset1:3\n" \
    "v_mfma_f32_16x16x32_bf16 v[172:175], v[40:43], v[80:83], v[172:175]\n" \
    "ds_read_b128 v[152:155], v214 offset:3072\n" \
    "v_mfma_f32_16x16x32_bf16 v[184:187], v[32:35], v[80:83], v[184:187]\n" \
    "ds_read_b128 v[140:143], v214 offset:1536\n" \
    "v_mfma_f32_16x16x32_bf16 v[188:191], v[44:47], v[80:83], v[188:191]\n" \
    "ds_read2_b32 v[104:105], v235 offset0:0 offset1:1\n" \
    "v_mfma_f32_16x16x32_bf16 v[168:171], v[40:43], v[76:79], v[168:171]\n" \
    "ds_read2_b32 v[106:107], v235 offset0:2 offset1:3\n" \
    "v_mfma_f32_16x16x32_bf16 v[172:175], v[44:47], v[76:79], v[172:175]\n" \
    "ds_read_b128 v[148:151], v214 offset:2560\n" \
    "v_mfma_f32_16x16x32_bf16 v[180:183], v[32:35], v[76:79], v[180:183]\n" \
    "ds_read2_b32 v[108:109], v236 offset0:0 offset1:1\n" \
    "v_mfma_f32_16x16x32_bf16 v[184:187], v[36:39], v[76:79], v[184:187]\n" \
    "s_waitcnt lgkmcnt(15)\n" \
    "v_mfma_f32_16x16x32_bf16 v[188:191], v[48:51], v[76:79], v[188:191]\n" \
    "ds_read2_b32 v[110:111], v236 offset0:2 offset1:3\n" \
    "v_mfma_f32_16x16x32_bf16 v[164:167], v[32:35], v[72:75], v[164:167]\n" \
    "ds_read_b128 v[136:139], v214 offset:1024\n" \
    "v_mfma_f32_16x16x32_bf16 v[168:171], v[44:47], v[72:75], v[168:171]\n" \
    "ds_read2_b32 v[112:113], v237 offset0:0 offset1:1\n" \
    "v_mfma_f32_16x16x32_bf16 v[172:175], v[48:51], v[72:75], v[172:175]\n" \
    "ds_read2_b32 v[114:115], v237 offset0:2 offset1:3\n" \
    "v_mfma_f32_16x16x32_bf16 v[180:183], v[36:39], v[72:75], v[180:183]\n" \
    "ds_read2_b32 v[116:117], v248 offset0:0 offset1:1\n" \
    "v_mfma_f32_16x16x32_bf16 v[184:187], v[40:43], v[72:75], v[184:187]\n" \
    "ds_read2_b32 v[118:119], v248 offset0:2 offset1:3\n" \
    "v_mfma_f32_16x16x32_bf16 v[188:191], v[52:55], v[72:75], v[188:191]\n" \
    "ds_read_b128 v[132:135], v214 offset:512\n" \
    "v_mfma_f32_16x16x32_bf16 v[160:163], v[32:35], v[68:71], v[160:163]\n" \
    "ds_read2_b32 v[120:121], v249 offset0:0 offset1:1\n" \
    "v_mfma_f32_16x16x32_bf16 v[164:167], v[36:39], v[68:71], v[164:167]\n" \
    "ds_read2_b32 v[122:123], v249 offset0:2 offset1:3\n" \
    "v_mfma_f32_16x16x32_bf16 v[168:171], v[48:51], v[68:71], v[168:171]\n" \
    "ds_read_b128 v[128:131], v214 offset:0\n" \
    "v_mfma_f32_16x16x32_bf16 v[172:175], v[52:55], v[68:71], v[172:175]\n" \
    "ds_read2_b32 v[124:125], v250 offset0:0 offset1:1\n" \
    "v_mfma_f32_16x16x32_bf16 v[180:183], v[40:43], v[68:71], v[180:183]\n" \
    "ds_read2_b32 v[126:127], v250 offset0:2 offset1:3\n" \
    "v_mov_b32 v241, 3\n" \
    "ds_write_b32 v226, v241 offset:80\n" \
    "v_mfma_f32_16x16x32_bf16 v[184:187], v[44:47], v[68:71], v[184:187]\n" \
    "v_mfma_f32_16x16x32_bf16 v[188:191], v[56:59], v[68:71], v[188:191]\n" \
    "s_waitcnt lgkmcnt(15)\n" \
    "v_mfma_f32_16x16x32_bf16 v[160:163], v[36:39], v[64:67], v[160:163]\n" \
    "v_mfma_f32_16x16x32_bf16 v[164:167], v[40:43], v[64:67], v[164:167]\n" \
    "v_mfma_f32_16x16x32_bf16 v[168:171], v[52:55], v[64:67], v[168:171]\n" \
    "v_mfma_f32_16x16x32_bf16 v[172:175], v[56:59], v[64:67], v[172:175]\n" \
    "v_mfma_f32_16x16x32_bf16 v[176:179], v[32:35], v[64:67], v[176:179]\n" \
    "v_mfma_f32_16x16x32_bf16 v[180:183], v[44:47], v[64:67], v[180:183]\n" \
    "v_mfma_f32_16x16x32_bf16 v[184:187], v[48:51], v[64:67], v[184:187]\n" \
    "v_mfma_f32_16x16x32_bf16 v[188:191], v[60:63], v[64:67], v[188:191]\n" \
    "v_mfma_f32_16x16x32_bf16 v[188:191], v[96:99], v[156:159], v[188:191]\n" \
    "v_mfma_f32_16x16x32_bf16 v[184:187], v[96:99], v[144:147], v[184:187]\n" \
    "v_mfma_f32_16x16x32_bf16 v[188:191], v[100:103], v[152:155], v[188:191]\n" \
    "v_mfma_f32_16x16x32_bf16 v[180:183], v[96:99], v[140:143], v[180:183]\n" \
    "s_waitcnt lgkmcnt(14)\n" \
    "v_mfma_f32_16x16x32_bf16 v[188:191], v[104:107], v[148:151], v[188:191]\n" \
    "v_mfma_f32_16x16x32_bf16 v[184:187], v[100:103], v[140:143], v[184:187]\n" \
    "s_waitcnt lgkmcnt(12)\n" \
    "v_mfma_f32_16x16x32_bf16 v[188:191], v[108:111], v[144:147], v[188:191]\n" \
    "s_waitcnt lgkmcnt(11)\n" \
    "v_mfma_f32_16x16x32_bf16 v[180:183], v[100:103], v[136:139], v[180:183]\n" \
    "s_waitcnt lgkmcnt(9)\n" \
    "v_mfma_f32_16x16x32_bf16 v[188:191], v[112:115], v[140:143], v[188:191]\n" \
    "v_mfma_f32_16x16x32_bf16 v[184:187], v[104:107], v[136:139], v[184:187]\n" \
    "s_waitcnt lgkmcnt(7)\n" \
    "v_mfma_f32_16x16x32_bf16 v[188:191], v[116:119], v[136:139], v[188:191]\n" \
    "s_waitcnt lgkmcnt(6)\n" \
    "v_mfma_f32_16x16x32_bf16 v[180:183], v[104:107], v[132:135], v[180:183]\n" \
    "v_mfma_f32_16x16x32_bf16 v[184:187], v[108:111], v[132:135], v[184:187]\n" \
    "s_waitcnt lgkmcnt(4)\n" \
    "v_mfma_f32_16x16x32_bf16 v[188:191], v[120:123], v[132:135], v[188:191]\n" \
    "s_waitcnt lgkmcnt(3)\n" \
    "v_mfma_f32_16x16x32_bf16 v[176:179], v[96:99], v[128:131], v[176:179]\n" \
    "v_mfma_f32_16x16x32_bf16 v[180:183], v[108:111], v[128:131], v[180:183]\n" \
    "v_mfma_f32_16x16x32_bf16 v[184:187], v[112:115], v[128:131], v[184:187]\n" \
    "s_waitcnt lgkmcnt(1)\n" \
    "v_mfma_f32_16x16x32_bf16 v[188:191], v[124:127], v[128:131], v[188:191]\n" \
    "v_mfma_f32_16x16x32_bf16 v[172:175], v[96:99], v[152:155], v[172:175]\n" \
    "v_mfma_f32_16x16x32_bf16 v[168:171], v[96:99], v[148:151], v[168:171]\n" \
    "Lp44:\n" \
    "ds_read_b32 v240, v227 offset:80\n" \
    "s_sub_u32 s70, s70, 1\n" \
    "s_cbranch_scc1 Labort\n" \
    "s_waitcnt lgkmcnt(0)\n" \
    "v_cmp_gt_u32 vcc, 3, v240\n" \
    "s_cbranch_vccnz Lp44\n" \
    "ds_write_b128 v199, v[176:179] offset:4096\n" \
    "ds_write_b128 v199, v[180:183] offset:5120\n" \
    "ds_write_b128 v199, v[184:187] offset:6144\n" \
    "ds_write_b128 v199, v[188:191] offset:7168\n" \
    "v_mov_b32 v241, 4\n" \
    "ds_write_b32 v226, v241 offset:80\n" \
    "v_mfma_f32_16x16x32_bf16 v[172:175], v[100:103], v[148:151], v[172:175]\n" \
    "v_mfma_f32_16x16x32_bf16 v[168:171], v[100:103], v[144:147], v[168:171]\n" \
    "v_mfma_f32_16x16x32_bf16 v[172:175], v[104:107], v[144:147], v[172:175]\n" \
    "v_mfma_f32_16x16x32_bf16 v[168:171], v[104:107], v[140:143], v[168:171]\n" \
    "v_mfma_f32_16x16x32_bf16 v[172:175], v[108:111], v[140:143], v[172:175]\n" \
    "v_mfma_f32_16x16x32_bf16 v[164:167], v[96:99], v[136:139], v[164:167]\n" \
    "v_mfma_f32_16x16x32_bf16 v[168:171], v[108:111], v[136:139], v[168:171]\n" \
    "v_mfma_f32_16x16x32_bf16 v[172:175], v[112:115], v[136:139], v[172:175]\n" \
    "v_mfma_f32_16x16x32_bf16 v[160:163], v[96:99], v[132:135], v[160:163]\n" \
    "v_mfma_f32_16x16x32_bf16 v[164:167], v[100:103], v[132:135], v[164:167]\n" \
    "v_mfma_f32_16x16x32_bf16 v[168:171], v[112:115], v[132:135], v[168:171]\n" \
    "v_mfma_f32_16x16x32_bf16 v[172:175], v[116:119], v[132:135], v[172:175]\n" \
    "v_mfma_f32_16x16x32_bf16 v[160:163], v[100:103], v[128:131], v[160:163]\n" \
    "v_mfma_f32_16x16x32_bf16 v[164:167], v[104:107], v[128:131], v[164:167]\n" \
    "v_mfma_f32_16x16x32_bf16 v[168:171], v[116:119], v[128:131], v[168:171]\n" \
    "v_mfma_f32_16x16x32_bf16 v[172:175], v[120:123], v[128:131], v[172:175]\n" \
    "Lp45:\n" \
    "ds_read_b32 v240, v227 offset:80\n" \
    "s_sub_u32 s70, s70, 1\n" \
    "s_cbranch_scc1 Labort\n" \
    "s_waitcnt lgkmcnt(0)\n" \
    "v_cmp_gt_u32 vcc, 4, v240\n" \
    "s_cbranch_vccnz Lp45\n" \
    "ds_read_b128 v[48:51], v199 offset:0\n" \
    "ds_read_b128 v[52:55], v199 offset:1024\n" \
    "ds_read_b128 v[56:59], v199 offset:2048\n" \
    "ds_read_b128 v[60:63], v199 offset:3072\n" \
    "v_mov_b32 v241, 6\n" \
    "ds_write_b32 v226, v241 offset:80\n" \
    "v_mov_b32 v17, v244\n" \
    "v_mov_b32 v19, v244\n" \
    "v_mov_b32 v21, v244\n" \
    "v_mov_b32 v23, v244\n" \
    "v_mov_b32 v25, v244\n" \
    "v_mov_b32 v27, v244\n" \
    "v_mov_b32 v29, v244\n" \
    "v_mov_b32 v31, v244\n" \
    "s_waitcnt lgkmcnt(4)\n" \
    "v_add_f32 v160, v160, v48\n" \
    "v_add_f32 v161, v161, v49\n" \
    "v_add_f32 v162, v162, v50\n" \
    "v_add_f32 v163, v163, v51\n" \
    "v_cvt_pk_bf16_f32 v32, v160, v161\n" \
    "v_cvt_pk_bf16_f32 v33, v162, v163\n" \
    "ds_bpermute_b32 v16, v215, v32\n" \
    "ds_bpermute_b32 v18, v215, v33\n" \
    "s_waitcnt lgkmcnt(5)\n" \
    "v_add_f32 v164, v164, v52\n" \
    "v_add_f32 v165, v165, v53\n" \
    "v_add_f32 v166, v166, v54\n" \
    "v_add_f32 v167, v167, v55\n" \
    "v_cvt_pk_bf16_f32 v34, v164, v165\n" \
    "v_cvt_pk_bf16_f32 v35, v166, v167\n" \
    "ds_bpermute_b32 v20, v215, v34\n" \
    "ds_bpermute_b32 v22, v215, v35\n" \
    "s_waitcnt lgkmcnt(6)\n" \
    "v_add_f32 v168, v168, v56\n" \
    "v_add_f32 v169, v169, v57\n" \
    "v_add_f32 v170, v170, v58\n" \
    "v_add_f32 v171, v171, v59\n" \
    "v_cvt_pk_bf16_f32 v36, v168, v169\n" \
    "v_cvt_pk_bf16_f32 v37, v170, v171\n" \
    "ds_bpermute_b32 v24, v215, v36\n" \
    "ds_bpermute_b32 v26, v215, v37\n" \
    "s_waitcnt lgkmcnt(7)\n" \
    "v_add_f32 v172, v172, v60\n" \
    "v_add_f32 v173, v173, v61\n" \
    "v_add_f32 v174, v174, v62\n" \
    "v_add_f32 v175, v175, v63\n" \
    "v_cvt_pk_bf16_f32 v38, v172, v173\n" \
    "v_cvt_pk_bf16_f32 v39, v174, v175\n" \
    "ds_bpermute_b32 v28, v215, v38\n" \
    "ds_bpermute_b32 v30, v215, v39\n" \
    "s_setprio 3\n" \
    "s_add_u32 s4, s34, 526848\n" \
    "s_waitcnt lgkmcnt(6)\n" \
    "buffer_store_dwordx4 v[16:19], v216, s[24:27], s4 offen sc1\n" \
    "s_add_u32 s4, s34, 1053696\n" \
    "s_waitcnt lgkmcnt(4)\n" \
    "buffer_store_dwordx4 v[20:23], v216, s[24:27], s4 offen sc1\n" \
    "s_add_u32 s4, s34, 2634240\n" \
    "s_waitcnt lgkmcnt(2)\n" \
    "buffer_store_dwordx4 v[24:27], v216, s[24:27], s4 offen sc1\n" \
    "s_add_u32 s4, s34, 3161088\n" \
    "s_waitcnt lgkmcnt(0)\n" \
    "buffer_store_dwordx4 v[28:31], v216, s[24:27], s4 offen sc1\n" \
    "Lconv2:\n" \
    "s_lshl_b32 s6, s10, 3\n" \
    "s_add_u32 s6, s6, 7667712\n" \
    "s_cmp_lg_u32 s31, 0\n" \
    "s_cbranch_scc1 Lnofy\n" \
    "s_mov_b64 exec, 1\n" \
    "buffer_store_dwordx2 v[244:245], off, s[24:27], s6 sc1\n" \
    "s_mov_b64 exec, -1\n" \
    "Lnofy:\n" \
    "s_setprio 0\n" \
    "global_load_dwordx4 v[32:35], v218, s[60:61]\n" \
    "v_mov_b32 v64, 0\n" \
    "v_mov_b32 v65, 0\n" \
    "v_mov_b32 v66, 0\n" \
    "v_mov_b32 v67, 0\n" \
    "v_mov_b32 v68, 0\n" \
    "v_mov_b32 v69, 0\n" \
    "v_mov_b32 v70, 0\n" \
    "v_mov_b32 v71, 0\n" \
    "v_mov_b32 v72, 0\n" \
    "v_mov_b32 v73, 0\n" \
    "v_mov_b32 v74, 0\n" \
    "v_mov_b32 v75, 0\n" \
    "v_mov_b32 v76, 0\n" \
    "v_mov_b32 v77, 0\n" \
    "v_mov_b32 v78, 0\n" \
    "v_mov_b32 v79, 0\n" \
    "Lp46:\n" \
    "ds_read_b32 v240, v238 offset:0\n" \
    "s_sub_u32 s70, s70, 1\n" \
    "s_cbranch_scc1 Labort\n" \
    "s_waitcnt lgkmcnt(0)\n" \
    "v_cmp_gt_u32 vcc, 20, v240\n" \
    "s_cbranch_vccz Ld47\n" \
    "s_sleep 2\n" \
    "s_branch Lp46\n" \
    "Ld47:\n" \
    "s_setprio 3\n" \
    "s_lshl2_add_u32 s4, s10, s31\n" \
    "s_cmp_eq_u32 s4, 1023\n" \
    "s_cbranch_scc0 Ld48\n" \
    "s_mov_b32 s4, 7667712\n" \
    "Lp49:\n" \
    "buffer_load_dwordx4 v[16:19], v211, s[24:27], s4 offen sc1\n" \
    "buffer_load_dwordx4 v[20:23], v211, s[24:27], s4 offen offset:1024 sc1\n" \
    "Ld48:\n" \
    "buffer_load_dwordx2 v[56:57], v217, s[24:27], s37 offen sc1\n" \
    "buffer_load_dwordx2 v[58:59], v217, s[24:27], s37 offen offset:32 sc1\n" \
    "buffer_load_dwordx4 v[40:43], v211, s[24:27], s35 offen offset:0 sc1\n" \
    "buffer_load_dwordx4 v[44:47], v211, s[24:27], s35 offen offset:1024 sc1\n" \
    "buffer_load_dwordx4 v[48:51], v211, s[24:27], s35 offen offset:2048 sc1\n" \
    "buffer_load_dwordx4 v[52:55], v211, s[24:27], s35 offen offset:3072 sc1\n" \
    "Lp51:\n" \
    "ds_read_b32 v240, v238 offset:80\n" \
    "s_sub_u32 s70, s70, 1\n" \
    "s_cbranch_scc1 Labort\n" \
    "s_waitcnt lgkmcnt(0)\n" \
    "v_cmp_gt_u32 vcc, 6, v240\n" \
    "s_cbranch_vccz Ld52\n" \
    "s_sleep 1\n" \
    "s_branch Lp51\n" \
    "Ld52:\n" \
    "s_waitcnt vmcnt(6)\n" \
    "v_lshlrev_b32 v96, 16, v32\n" \
    "v_and_b32 v97, 0xffff0000, v32\n" \
    "v_lshlrev_b32 v98, 16, v33\n" \
    "v_and_b32 v99, 0xffff0000, v33\n" \
    "v_lshlrev_b32 v100, 16, v34\n" \
    "v_and_b32 v101, 0xffff0000, v34\n" \
    "v_lshlrev_b32 v102, 16, v35\n" \
    "v_and_b32 v103, 0xffff0000, v35\n" \
    "s_waitcnt vmcnt(0)\n" \
    "Lk54:\n" \
    "v_bitop3_b32 v8, v41, v43, v244 bitop3:0x7e\n" \
    "v_bitop3_b32 v9, v45, v47, v244 bitop3:0x7e\n" \
    "v_bitop3_b32 v10, v49, v51, v244 bitop3:0x7e\n" \
    "v_bitop3_b32 v11, v53, v55, v244 bitop3:0x7e\n" \
    "v_bitop3_b32 v12, v57, v59, v244 bitop3:0x7e\n" \
    "v_or3_b32 v8, v8, v9, v10\n" \
    "v_or3_b32 v8, v8, v11, v12\n" \
    "v_cmp_ne_u32 vcc, 0, v8\n" \
    "s_cbranch_vccnz Ls53\n" \
    "ds_write2_b32 v219, v40, v42 offset0:0 offset1:1\n" \
    "ds_write2_b32 v219, v44, v46 offset0:128 offset1:129\n" \
    "ds_write2_b32 v220, v48, v50 offset0:0 offset1:1\n" \
    "ds_write2_b32 v220, v52, v54 offset0:128 offset1:129\n" \
    "v_mov_b32 v241, 7\n" \
    "ds_write_b32 v226, v241 offset:80\n" \
    "v_add_f32_dpp v56, v56, v56 quad_perm:[1,0,3,2] row_mask:0xf bank_mask:0xf\n" \
    "v_add_f32_dpp v58, v58, v58 quad_perm:[1,0,3,2] row_mask:0xf bank_mask:0xf\n" \
    "s_nop 0\n" \
    "v_add_f32_dpp v56, v56, v56 quad_perm:[2,3,0,1] row_mask:0xf bank_mask:0xf\n" \
    "v_add_f32_dpp v58, v58, v58 quad_perm:[2,3,0,1] row_mask:0xf bank_mask:0xf\n" \
    "s_nop 0\n" \
    "v_add_f32_dpp v56, v56, v56 row_half_mirror row_mask:0xf bank_mask:0xf\n" \
    "v_add_f32_dpp v58, v58, v58 row_half_mirror row_mask:0xf bank_mask:0xf\n" \
    "v_mul_f32 v56, 0xbfb8aa3b, v56\n" \
    "v_mul_f32 v58, 0xbfb8aa3b, v58\n" \
    "v_exp_f32 v56, v56\n" \
    "v_exp_f32 v58, v58\n" \
    "v_add_f32 v56, 1.0, v56\n" \
    "v_add_f32 v58, 1.0, v58\n" \
    "v_rcp_f32 v56, v56\n" \
    "v_rcp_f32 v58, v58\n" \
    "v_sub_f32 v57, 1.0, v56\n" \
    "v_sub_f32 v59, 1.0, v58\n" \
    "Lp55:\n" \
    "ds_read_b32 v240, v238 offset:48\n" \
    "s_sub_u32 s70, s70, 1\n" \
    "s_cbranch_scc1 Labort\n" \
    "s_waitcnt lgkmcnt(0)\n" \
    "v_cmp_gt_u32 vcc, 1, v240\n" \
    "s_cbranch_vccz Ld56\n" \
    "s_sleep 1\n" \
    "s_branch Lp55\n" \
    "Ld56:\n" \
    "ds_read_b128 v[128:131], v223 offset:8192\n" \
    "ds_read_b128 v[132:135], v223 offset:24576\n" \
    "ds_read_b128 v[136:139], v223 offset:40960\n" \
    "ds_read_b128 v[140:143], v223 offset:57344\n" \
    "ds_read_b128 v[144:147], v224 offset:8192\n" \
    "ds_read_b128 v[148:151], v224 offset:24576\n" \
    "ds_read_b128 v[152:155], v224 offset:40960\n" \
    "ds_read_b128 v[156:159], v224 offset:57344\n" \
    "s_waitcnt lgkmcnt(6)\n" \
    "v_add_f32 v128, v128, v132\n" \
    "v_add_f32 v129, v129, v133\n" \
    "v_add_f32 v130, v130, v134\n" \
    "v_add_f32 v131, v131, v135\n" \
    "s_waitcnt lgkmcnt(4)\n" \
    "v_add_f32 v136, v136, v140\n" \
    "v_add_f32 v137, v137, v141\n" \
    "v_add_f32 v138, v138, v142\n" \
    "v_add_f32 v139, v139, v143\n" \
    "v_add_f32 v128, v128, v136\n" \
    "v_add_f32 v129, v129, v137\n" \
    "v_add_f32 v130, v130, v138\n" \
    "v_add_f32 v131, v131, v139\n" \
    "s_waitcnt lgkmcnt(2)\n" \
    "v_add_f32 v144, v144, v148\n" \
    "v_add_f32 v145, v145, v149\n" \
    "v_add_f32 v146, v146, v150\n" \
    "v_add_f32 v147, v147, v151\n" \
    "s_waitcnt lgkmcnt(0)\n" \
    "v_add_f32 v152, v152, v156\n" \
    "v_add_f32 v153, v153, v157\n" \
    "v_add_f32 v154, v154, v158\n" \
    "v_add_f32 v155, v155, v159\n" \
    "v_add_f32 v144, v144, v152\n" \
    "v_add_f32 v145, v145, v153\n" \
    "v_add_f32 v146, v146, v154\n" \
    "v_add_f32 v147, v147, v155\n" \
    "Lp57:\n" \
    "ds_read_b32 v240, v238 offset:80\n" \
    "s_sub_u32 s70, s70, 1\n" \
    "s_cbranch_scc1 Labort\n" \
    "s_waitcnt lgkmcnt(0)\n" \
    "v_cmp_gt_u32 vcc, 7, v240\n" \
    "s_cbranch_vccnz Lp57\n" \
    "ds_read_u16_d16_hi v64, v221 offset:0\n" \
    "ds_read_u16_d16_hi v65, v221 offset:128\n" \
    "ds_read_u16_d16_hi v66, v221 offset:256\n" \
    "ds_read_u16_d16_hi v67, v221 offset:384\n" \
    "ds_read_u16_d16_hi v68, v221 offset:512\n" \
    "ds_read_u16_d16_hi v69, v221 offset:640\n" \
    "ds_read_u16_d16_hi v70, v221 offset:768\n" \
    "ds_read_u16_d16_hi v71, v221 offset:896\n" \
    "ds_read_u16_d16_hi v72, v222 offset:0\n" \
    "ds_read_u16_d16_hi v73, v222 offset:128\n" \
    "ds_read_u16_d16_hi v74, v222 offset:256\n" \
    "ds_read_u16_d16_hi v75, v222 offset:384\n" \
    "ds_read_u16_d16_hi v76, v222 offset:512\n" \
    "ds_read_u16_d16_hi v77, v222 offset:640\n" \
    "ds_read_u16_d16_hi v78, v222 offset:768\n" \
    "ds_read_u16_d16_hi v79, v222 offset:896\n" \
    "v_mul_f32 v104, v57, v96\n" \
    "v_mul_f32 v105, v57, v97\n" \
    "v_mul_f32 v106, v57, v98\n" \
    "v_mul_f32 v107, v57, v99\n" \
    "v_mul_f32 v108, v57, v100\n" \
    "v_mul_f32 v109, v57, v101\n" \
    "v_mul_f32 v110, v57, v102\n" \
    "v_mul_f32 v111, v57, v103\n" \
    "v_mul_f32 v112, v59, v96\n" \
    "v_mul_f32 v113, v59, v97\n" \
    "v_mul_f32 v114, v59, v98\n" \
    "v_mul_f32 v115, v59, v99\n" \
    "v_mul_f32 v116, v59, v100\n" \
    "v_mul_f32 v117, v59, v101\n" \
    "v_mul_f32 v118, v59, v102\n" \
    "v_mul_f32 v119, v59, v103\n" \
    "s_waitcnt lgkmcnt(8)\n" \
    "v_fma_f32 v64, v56, v64, v104\n" \
    "v_fma_f32 v65, v56, v65, v105\n" \
    "v_fma_f32 v66, v56, v66, v106\n" \
    "v_fma_f32 v67, v56, v67, v107\n" \
    "v_fma_f32 v68, v56, v68, v108\n" \
    "v_fma_f32 v69, v56, v69, v109\n" \
    "v_fma_f32 v70, v56, v70, v110\n" \
    "v_fma_f32 v71, v56, v71, v111\n" \
    "v_fmac_f32 v64, v57, v128\n" \
    "v_fmac_f32 v65, v57, v129\n" \
    "v_fmac_f32 v66, v57, v130\n" \
    "v_fmac_f32 v67, v57, v131\n" \
    "v_cvt_pk_bf16_f32 v32, v64, v65\n" \
    "v_cvt_pk_bf16_f32 v33, v66, v67\n" \
    "v_fmac_f32 v68, v57, v144\n" \
    "v_fmac_f32 v69, v57, v145\n" \
    "v_fmac_f32 v70, v57, v146\n" \
    "v_fmac_f32 v71, v57, v147\n" \
    "v_cvt_pk_bf16_f32 v34, v68, v69\n" \
    "v_cvt_pk_bf16_f32 v35, v70, v71\n" \
    "buffer_store_dwordx4 v[32:35], v225, s[64:67], s68 offen\n" \
    "s_waitcnt lgkmcnt(0)\n" \
    "v_fma_f32 v72, v58, v72, v112\n" \
    "v_fma_f32 v73, v58, v73, v113\n" \
    "v_fma_f32 v74, v58, v74, v114\n" \
    "v_fma_f32 v75, v58, v75, v115\n" \
    "v_fma_f32 v76, v58, v76, v116\n" \
    "v_fma_f32 v77, v58, v77, v117\n" \
    "v_fma_f32 v78, v58, v78, v118\n" \
    "v_fma_f32 v79, v58, v79, v119\n" \
    "s_lshl2_add_u32 s4, s10, s31\n" \
    "s_cmp_eq_u32 s4, 1023\n" \
    "s_cbranch_scc0 Ld58\n" \
    "s_mov_b32 s4, 7667712\n" \
    "s_branch Lk60\n" \
    "Lp59:\n" \
    "buffer_load_dwordx4 v[16:19], v211, s[24:27], s4 offen sc1\n" \
    "buffer_load_dwordx4 v[20:23], v211, s[24:27], s4 offen offset:1024 sc1\n" \
    "s_sub_u32 s70, s70, 1\n" \
    "s_cbranch_scc1 Labort\n" \
    "s_waitcnt vmcnt(0)\n" \
    "Lk60:\n" \
    "v_cmp_ne_u64 vcc, v[16:17], v[244:245]\n" \
    "v_cmp_ne_u64 s[6:7], v[18:19], v[244:245]\n" \
    "s_or_b64 vcc, vcc, s[6:7]\n" \
    "v_cmp_ne_u64 s[6:7], v[20:21], v[244:245]\n" \
    "s_or_b64 vcc, vcc, s[6:7]\n" \
    "v_cmp_ne_u64 s[6:7], v[22:23], v[244:245]\n" \
    "s_or_b64 vcc, vcc, s[6:7]\n" \
    "s_cbranch_vccnz Lp59\n" \
    "s_mov_b32 s6, 0x80B583EC\n" \
    "s_mov_b32 s7, 0x61C88646\n" \
    "v_lshl_add_u64 v[8:9], v[244:245], 0, s[6:7]\n" \
    "s_mov_b32 s4, 7831552\n" \
    "s_mov_b64 exec, 1\n" \
    "buffer_store_dwordx2 v[8:9], off, s[24:27], s4 sc1\n" \
    "s_mov_b64 exec, -1\n" \
    "Ld58:\n" \
    "Lp61:\n" \
    "ds_read_b32 v240, v238 offset:48\n" \
    "s_sub_u32 s70, s70, 1\n" \
    "s_cbranch_scc1 Labort\n" \
    "s_waitcnt lgkmcnt(0)\n" \
    "v_cmp_gt_u32 vcc, 2, v240\n" \
    "s_cbranch_vccnz Lp61\n" \
    "ds_read_b128 v[128:131], v223 offset:0\n" \
    "ds_read_b128 v[132:135], v223 offset:16384\n" \
    "ds_read_b128 v[136:139], v223 offset:32768\n" \
    "ds_read_b128 v[140:143], v223 offset:49152\n" \
    "ds_read_b128 v[144:147], v224 offset:0\n" \
    "ds_read_b128 v[148:151], v224 offset:16384\n" \
    "ds_read_b128 v[152:155], v224 offset:32768\n" \
    "ds_read_b128 v[156:159], v224 offset:49152\n" \
    "s_add_u32 s68, s68, 32768\n" \
    "s_waitcnt lgkmcnt(6)\n" \
    "v_add_f32 v128, v128, v132\n" \
    "v_add_f32 v129, v129, v133\n" \
    "v_add_f32 v130, v130, v134\n" \
    "v_add_f32 v131, v131, v135\n" \
    "s_waitcnt lgkmcnt(4)\n" \
    "v_add_f32 v136, v136, v140\n" \
    "v_add_f32 v137, v137, v141\n" \
    "v_add_f32 v138, v138, v142\n" \
    "v_add_f32 v139, v139, v143\n" \
    "v_add_f32 v128, v128, v136\n" \
    "v_add_f32 v129, v129, v137\n" \
    "v_add_f32 v130, v130, v138\n" \
    "v_add_f32 v131, v131, v139\n" \
    "v_fmac_f32 v72, v59, v128\n" \
    "v_fmac_f32 v73, v59, v129\n" \
    "v_fmac_f32 v74, v59, v130\n" \
    "v_fmac_f32 v75, v59, v131\n" \
    "v_cvt_pk_bf16_f32 v36, v72, v73\n" \
    "v_cvt_pk_bf16_f32 v37, v74, v75\n" \
    "s_waitcnt lgkmcnt(2)\n" \
    "v_add_f32 v144, v144, v148\n" \
    "v_add_f32 v145, v145, v149\n" \
    "v_add_f32 v146, v146, v150\n" \
    "v_add_f32 v147, v147, v151\n" \
    "s_waitcnt lgkmcnt(0)\n" \
    "v_add_f32 v152, v152, v156\n" \
    "v_add_f32 v153, v153, v157\n" \
    "v_add_f32 v154, v154, v158\n" \
    "v_add_f32 v155, v155, v159\n" \
    "v_add_f32 v144, v144, v152\n" \
    "v_add_f32 v145, v145, v153\n" \
    "v_add_f32 v146, v146, v154\n" \
    "v_add_f32 v147, v147, v155\n" \
    "v_fmac_f32 v76, v59, v144\n" \
    "v_fmac_f32 v77, v59, v145\n" \
    "v_fmac_f32 v78, v59, v146\n" \
    "v_fmac_f32 v79, v59, v147\n" \
    "v_cvt_pk_bf16_f32 v38, v76, v77\n" \
    "v_cvt_pk_bf16_f32 v39, v78, v79\n" \
    "buffer_store_dwordx4 v[36:39], v225, s[64:67], s68 offen\n" \
    "s_endpgm\n" \
    "Ls2:\n" \
    "ds_read_b32 v240, v238\n" \
    "s_sub_u32 s70, s70, 1\n" \
    "s_cbranch_scc1 Labort\n" \
    "s_waitcnt lgkmcnt(0)\n" \
    "v_cmp_gt_u32 vcc, 2, v240\n" \
    "s_cbranch_vccnz Ls2\n" \
    "s_branch Lb3\n" \
    "Ls5:\n" \
    "ds_read_b32 v240, v238\n" \
    "s_sub_u32 s70, s70, 1\n" \
    "s_cbranch_scc1 Labort\n" \
    "s_waitcnt lgkmcnt(0)\n" \
    "v_cmp_gt_u32 vcc, 3, v240\n" \
    "s_cbranch_vccnz Ls5\n" \
    "s_branch Lb6\n" \
    "Ls9:\n" \
    "ds_read_b32 v240, v238\n" \
    "s_sub_u32 s70, s70, 1\n" \
    "s_cbranch_scc1 Labort\n" \
    "s_waitcnt lgkmcnt(0)\n" \
    "v_cmp_gt_u32 vcc, 5, v240\n" \
    "s_cbranch_vccnz Ls9\n" \
    "s_branch Lb10\n" \
    "Ls12:\n" \
    "ds_read_b32 v240, v238\n" \
    "s_sub_u32 s70, s70, 1\n" \
    "s_cbranch_scc1 Labort\n" \
    "s_waitcnt lgkmcnt(0)\n" \
    "v_cmp_gt_u32 vcc, 7, v240\n" \
    "s_cbranch_vccnz Ls12\n" \
    "s_branch Lb13\n" \
    "Ls14:\n" \
    "v_mov_b32 v244, 163328\n" \
    "ds_read_b96 v[244:246], v244 offset:64\n" \
    "s_sub_u32 s70, s70, 1\n" \
    "s_cbranch_scc1 Labort\n" \
    "s_waitcnt lgkmcnt(0)\n" \
    "v_cmp_eq_u32 vcc, 0, v246\n" \
    "s_cbranch_vccnz Ls14\n" \
    "s_branch Lb15\n" \
    "Ls16:\n" \
    "ds_read_b32 v240, v238\n" \
    "s_sub_u32 s70, s70, 1\n" \
    "s_cbranch_scc1 Labort\n" \
    "s_waitcnt lgkmcnt(0)\n" \
    "v_cmp_gt_u32 vcc, 9, v240\n" \
    "s_cbranch_vccnz Ls16\n" \
    "s_branch Lb17\n" \
    "Ls18:\n" \
    "ds_read_b32 v240, v238\n" \
    "s_sub_u32 s70, s70, 1\n" \
    "s_cbranch_scc1 Labort\n" \
    "s_waitcnt lgkmcnt(0)\n" \
    "v_cmp_gt_u32 vcc, 10, v240\n" \
    "s_cbranch_vccnz Ls18\n" \
    "s_branch Lb19\n" \
    "Ls20:\n" \
    "ds_read_b32 v240, v238\n" \
    "s_sub_u32 s70, s70, 1\n" \
    "s_cbranch_scc1 Labort\n" \
    "s_waitcnt lgkmcnt(0)\n" \
    "v_cmp_gt_u32 vcc, 11, v240\n" \
    "s_cbranch_vccnz Ls20\n" \
    "s_branch Lb21\n" \
    "Ls22:\n" \
    "ds_read_b32 v240, v238\n" \
    "s_sub_u32 s70, s70, 1\n" \
    "s_cbranch_scc1 Labort\n" \
    "s_waitcnt lgkmcnt(0)\n" \
    "v_cmp_gt_u32 vcc, 13, v240\n" \
    "s_cbranch_vccnz Ls22\n" \
    "s_branch Lb23\n" \
    "Ls24:\n" \
    "ds_read_b32 v240, v238\n" \
    "s_sub_u32 s70, s70, 1\n" \
    "s_cbranch_scc1 Labort\n" \
    "s_waitcnt lgkmcnt(0)\n" \
    "v_cmp_gt_u32 vcc, 14, v240\n" \
    "s_cbranch_vccnz Ls24\n" \
    "s_branch Lb25\n" \
    "Ls26:\n" \
    "s_sleep 4\n" \
    "buffer_load_dwordx2 v[224:225], v226, s[24:27], s77 offen sc1\n" \
    "s_sub_u32 s70, s70, 1\n" \
    "s_cbranch_scc1 Labort\n" \
    "s_waitcnt vmcnt(0)\n" \
    "v_cmp_ne_u64 vcc, v[224:225], v[244:245]\n" \
    "s_cbranch_vccnz Ls26\n" \
    "s_branch Lb27\n" \
    "Ls28:\n" \
    "ds_read_b32 v240, v238\n" \
    "s_sub_u32 s70, s70, 1\n" \
    "s_cbranch_scc1 Labort\n" \
    "s_waitcnt lgkmcnt(0)\n" \
    "v_cmp_gt_u32 vcc, 18, v240\n" \
    "s_cbranch_vccnz Ls28\n" \
    "s_branch Lb29\n" \
    "Ls30:\n" \
    "s_sleep 4\n" \
    "buffer_load_dwordx2 v[224:225], v226, s[24:27], s77 offen sc1\n" \
    "s_sub_u32 s70, s70, 1\n" \
    "s_cbranch_scc1 Labort\n" \
    "s_waitcnt vmcnt(0)\n" \
    "v_cmp_ne_u64 vcc, v[224:225], v[244:245]\n" \
    "s_cbranch_vccnz Ls30\n" \
    "s_branch Lb31\n" \
    "Ls32:\n" \
    "v_mov_b32 v244, 163328\n" \
    "ds_read_b96 v[244:246], v244 offset:64\n" \
    "s_sub_u32 s70, s70, 1\n" \
    "s_cbranch_scc1 Labort\n" \
    "s_waitcnt lgkmcnt(0)\n" \
    "v_cmp_eq_u32 vcc, 0, v246\n" \
    "s_cbranch_vccnz Ls32\n" \
    "s_branch Lb33\n" \
    "Ls34:\n" \
    "v_mov_b32 v244, 163328\n" \
    "ds_read_b96 v[244:246], v244 offset:64\n" \
    "s_sub_u32 s70, s70, 1\n" \
    "s_cbranch_scc1 Labort\n" \
    "s_waitcnt lgkmcnt(0)\n" \
    "v_cmp_eq_u32 vcc, 0, v246\n" \
    "s_cbranch_vccnz Ls34\n" \
    "s_branch Lb35\n" \
    "Ls39:\n" \
    "s_sleep 4\n" \
    "buffer_load_dwordx2 v[128:129], v212, s[24:27], s11 offen sc1\n" \
    "s_sub_u32 s70, s70, 1\n" \
    "s_cbranch_scc1 Labort\n" \
    "s_waitcnt vmcnt(0)\n" \
    "s_branch Lk40\n" \
    "Ls53:\n" \
    "s_sleep 2\n" \
    "s_mov_b64 exec, vcc\n" \
    "buffer_load_dwordx2 v[56:57], v217, s[24:27], s37 offen sc1\n" \
    "buffer_load_dwordx2 v[58:59], v217, s[24:27], s37 offen offset:32 sc1\n" \
    "buffer_load_dwordx4 v[40:43], v211, s[24:27], s35 offen offset:0 sc1\n" \
    "buffer_load_dwordx4 v[44:47], v211, s[24:27], s35 offen offset:1024 sc1\n" \
    "buffer_load_dwordx4 v[48:51], v211, s[24:27], s35 offen offset:2048 sc1\n" \
    "buffer_load_dwordx4 v[52:55], v211, s[24:27], s35 offen offset:3072 sc1\n" \
    "s_mov_b64 exec, -1\n" \
    "s_sub_u32 s70, s70, 1\n" \
    "s_cbranch_scc1 Labort\n" \
    "s_waitcnt vmcnt(0)\n" \
    "s_branch Lk54\n" \
    "Lcend:\n" \
    ""
#define MK_CLOBBER \
    "s4", "s5", "s6", "s7", "s8", "s9", "s10", "s11", "s12", "s13", "s14", "s15", "s16", "s17", "s18", "s19", \
    "s20", "s21", "s22", "s23", "s24", "s25", "s26", "s27", "s28", "s29", "s30", "s31", "s33", "s34", "s35", "s36", \
    "s37", "s38", "s39", "s40", "s41", "s42", "s43", "s44", "s45", "s46", "s47", "s48", "s49", "s50", "s51", "s52", \
    "s53", "s54", "s55", "s56", "s57", "s58", "s59", "s60", "s61", "s62", "s63", "s64", "s65", "s66", "s67", "s68", \
    "s69", "s70", "s71", "s72", "s73", "s74", "s75", "s76", "s77", "s78", "s79", "s80", "s81", "s82", "s83", "s84", \
    "s85", "s86", "s87", "s88", "s89", "s90", "s91", "s92", "s93", "s94", "s95", "s96", "s97", "s98", "s99", "v1", \
    "v2", "v3", "v4", "v5", "v6", "v7", "v8", "v9", "v10", "v11", "v12", "v13", "v14", "v15", "v16", "v17", \
    "v18", "v19", "v20", "v21", "v22", "v23", "v24", "v25", "v26", "v27", "v28", "v29", "v30", "v31", "v32", "v33", \
    "v34", "v35", "v36", "v37", "v38", "v39", "v40", "v41", "v42", "v43", "v44", "v45", "v46", "v47", "v48", "v49", \
    "v50", "v51", "v52", "v53", "v54", "v55", "v56", "v57", "v58", "v59", "v60", "v61", "v62", "v63", "v64", "v65", \
    "v66", "v67", "v68", "v69", "v70", "v71", "v72", "v73", "v74", "v75", "v76", "v77", "v78", "v79", "v80", "v81", \
    "v82", "v83", "v84", "v85", "v86", "v87", "v88", "v89", "v90", "v91", "v92", "v93", "v94", "v95", "v96", "v97", \
    "v98", "v99", "v100", "v101", "v102", "v103", "v104", "v105", "v106", "v107", "v108", "v109", "v110", "v111", "v112", "v113", \
    "v114", "v115", "v116", "v117", "v118", "v119", "v120", "v121", "v122", "v123", "v124", "v125", "v126", "v127", "v128", "v129", \
    "v130", "v131", "v132", "v133", "v134", "v135", "v136", "v137", "v138", "v139", "v140", "v141", "v142", "v143", "v144", "v145", \
    "v146", "v147", "v148", "v149", "v150", "v151", "v152", "v153", "v154", "v155", "v156", "v157", "v158", "v159", "v160", "v161", \
    "v162", "v163", "v164", "v165", "v166", "v167", "v168", "v169", "v170", "v171", "v172", "v173", "v174", "v175", "v176", "v177", \
    "v178", "v179", "v180", "v181", "v182", "v183", "v184", "v185", "v186", "v187", "v188", "v189", "v190", "v191", "v192", "v193", \
    "v194", "v195", "v196", "v197", "v198", "v199", "v200", "v201", "v202", "v203", "v204", "v205", "v206", "v207", "v208", "v209", \
    "v210", "v211", "v212", "v213", "v214", "v215", "v216", "v217", "v218", "v219", "v220", "v221", "v222", "v223", "v224", "v225", \
    "v226", "v227", "v228", "v229", "v230", "v231", "v232", "v233", "v234", "v235", "v236", "v237", "v238", "v239", "v240", "v241", \
    "v242", "v243", "v244", "v245", "v246", "v247", "v248", "v249", "v250", "v251", "v252", "v253", "v254", "v255", "vcc", "scc", \
    "memory"

__global__ __launch_bounds__(512, 2) void mk(Prm prm) {
    __shared__ __attribute__((aligned(16))) char smem[163840];
    const unsigned long long ka = (unsigned long long)__builtin_amdgcn_kernarg_segment_ptr();
    const unsigned lb = (unsigned)(uintptr_t)(__attribute__((address_space(3))) char*)smem;
    asm volatile(MK_ASM :: "s"(ka), "s"(blockIdx.x), "v"(threadIdx.x), "i"(lb) : MK_CLOBBER);
}

extern "C" void kernel_launch(void* const* d_in, const int* in_sizes, int n_in,
                              void* d_out, int out_size, void* d_ws, size_t ws_size,
                              hipStream_t stream) {
    Prm p;
    p.x = Srd{d_in[0], 2048u * 1024u, 0x20000u};
    p.w1 = Srd{d_in[10], 512u * 3584u, 0x20000u};
    p.w2 = Srd{d_in[12], 1792u * 1024u, 0x20000u};
    p.ws = Srd{d_ws, WS_USED, 0x20000u};
    p.b1 = d_in[11]; p.out = d_out;
    p.ar = d_in[1]; p.ai = d_in[2]; p.br = d_in[3]; p.bi = d_in[4]; p.cr = d_in[5]; p.ci = d_in[6];
    p.ldt = d_in[7]; p.dsk = d_in[8]; p.b2 = d_in[13]; p.gw = d_in[14];
    mk<<<256, 512, 0, stream>>>(p);
}
```
